```python
import torch

def _softmax(x, axis=-1):
    return torch.softmax(x, dim=axis)
def _gelu(x):
    return torch.nn.functional.gelu(x, approximate="tanh")
def _silu(x):
    return torch.nn.functional.silu(x)
def _sigmoid(x):
    return torch.sigmoid(x)
def _layernorm(x, axis=-1, eps=1e-5):
    m = x.mean(dim=axis, keepdim=True)
    v = x.var(dim=axis, keepdim=True, unbiased=False)
    return (x - m) / torch.sqrt(v + eps)
def _scatter_add(tgt, idx, src, axis=0):
    return tgt.clone().index_add_(axis, idx, src)
def _segment_sum(src, seg, n):
    out = torch.zeros((n,) + src.shape[1:], dtype=src.dtype)
    return out.index_add_(0, seg, src)
def _pad(x, pw):
    flat = tuple(v for pair in reversed(pw) for v in pair)
    return torch.nn.functional.pad(x, flat)

kernel_name = 'standalone__mha_softmax__near_ridge'

def setup_inputs():

    _w = (lambda a: (lambda t: t.to(torch.bfloat16) if t.is_floating_point() else t)(torch.as_tensor(a)))
    B, H, Sq, Sk, D = 8, 32, 512, 512, 192
    return {
        "q": _w((lambda *s: torch.randn(*s, dtype=torch.bfloat16) * 0.8)(B, H, Sq, D)),
        "k": _w((lambda *s: torch.randn(*s, dtype=torch.bfloat16) * 0.8)(B, H, Sk, D)),
        "v": _w((lambda *s: torch.randn(*s, dtype=torch.bfloat16) * 0.05)(B, H, Sk, D)),
    }

def reference(q, k, v):
    scale = q.shape[-1] ** -0.5
    scores = torch.einsum("bhqd,bhkd->bhqk", q, k) * scale
    p = _softmax(scores, axis=-1)
    return torch.einsum("bhqk,bhkd->bhqd", p, v)
```

```cpp
#include <hip/hip_runtime.h>
#include <hip/hip_bf16.h>
#include <stdint.h>

typedef short bf16x8 __attribute__((ext_vector_type(8)));
typedef short s16x4 __attribute__((ext_vector_type(4)));
typedef float f32x16 __attribute__((ext_vector_type(16)));
typedef unsigned u32x4 __attribute__((ext_vector_type(4)));
typedef unsigned u32x2 __attribute__((ext_vector_type(2)));
typedef float f32x2 __attribute__((ext_vector_type(2)));
typedef __bf16 bf16x2_t __attribute__((ext_vector_type(2)));

constexpr int NB = 8, NH = 32, SQ = 512, SK = 512, D = 192;
constexpr float CSC = 0.07216878364870322f * 1.4426950408889634f;

__device__ __forceinline__ unsigned cvtpk(float lo, float hi) {
  f32x2 v = {lo, hi}; bf16x2_t b = __builtin_convertvector(v, bf16x2_t); return __builtin_bit_cast(unsigned, b);
}

__global__ __launch_bounds__(256) void attn_base(const uint16_t* __restrict__ Q, const uint16_t* __restrict__ K,
                                                 const uint16_t* __restrict__ V, uint16_t* __restrict__ O) {
  __shared__ __attribute__((aligned(16))) char lds[2 * 12288];
  char* Kl = lds; char* Vl = lds + 12288;
  const int tid = threadIdx.x, lane = tid & 63, w = tid >> 6, r = lane & 31, h = lane >> 5;
  const int bid = blockIdx.x;
  const int xcd = bid & 7, j = bid >> 3;
  const int head = xcd * 32 + (j >> 2), qblk = j & 3;
  const uint16_t* Qh = Q + ((size_t)head * SQ + qblk * 128 + w * 32) * D;
  const uint16_t* Kh = K + (size_t)head * SK * D;
  const uint16_t* Vh = V + (size_t)head * SK * D;
  bf16x8 qf[12];
#pragma unroll
  for (int d0 = 0; d0 < 12; ++d0) qf[d0] = *(const bf16x8*)(Qh + r * D + d0 * 16 + h * 8);
  f32x16 o[6];
#pragma unroll
  for (int i = 0; i < 6; ++i) o[i] = f32x16{};
  float m = -1e30f, l = 0.f;
  for (int t = 0; t < SK / 32; ++t) {
    __syncthreads();
#pragma unroll
    for (int k = 0; k < 3; ++k) {
      const int n = k * 256 + tid; const int L = n >> 3, c = n & 7, key = L / 3;
      const int ck = c ^ ((key >> 1) & 7);
      const int cv = c ^ (4 * ((key >> 1) & 1));
      *(u32x4*)(Kl + n * 16) = *(const u32x4*)((const char*)(Kh + t * 32 * D) + L * 128 + ck * 16);
      *(u32x4*)(Vl + n * 16) = *(const u32x4*)((const char*)(Vh + t * 32 * D) + L * 128 + cv * 16);
    }
    __syncthreads();
    f32x16 s = f32x16{};
#pragma unroll
    for (int d0 = 0; d0 < 12; ++d0) {
      const int jj = d0 >> 2, c = 2 * (d0 & 3) + h;
      bf16x8 kf = *(const bf16x8*)(Kl + 128 * (3 * r + jj) + 16 * (c ^ ((r >> 1) & 7)));
      s = __builtin_amdgcn_mfma_f32_32x32x16_bf16(kf, qf[d0], s, 0, 0, 0);
    }
    float mx = s[0];
#pragma unroll
    for (int i = 1; i < 16; ++i) mx = fmaxf(mx, s[i]);
    mx = fmaxf(mx, __shfl_xor(mx, 32));
    const float mn = fmaxf(m, mx);
    const float f = __builtin_amdgcn_exp2f((m - mn) * CSC);
    m = mn;
    const float nmc = -mn * CSC;
    float ps = 0.f;
#pragma unroll
    for (int i = 0; i < 16; ++i) { s[i] = __builtin_amdgcn_exp2f(fmaf(s[i], CSC, nmc)); ps += s[i]; }
    l = l * f + ps;
#pragma unroll
    for (int i = 0; i < 6; ++i) o[i] *= f;
    bf16x8 pf[2];
#pragma unroll
    for (int ks = 0; ks < 2; ++ks) {
      u32x4 wv = {cvtpk(s[8 * ks], s[8 * ks + 1]), cvtpk(s[8 * ks + 2], s[8 * ks + 3]), cvtpk(s[8 * ks + 4], s[8 * ks + 5]), cvtpk(s[8 * ks + 6], s[8 * ks + 7])};
      pf[ks] = __builtin_bit_cast(bf16x8, wv);
    }
    const int blk = (lane >> 4) & 1, q = (lane >> 2) & 3, p = lane & 3;
#pragma unroll
    for (int ks = 0; ks < 2; ++ks)
#pragma unroll
      for (int db = 0; db < 6; ++db) {
        const int jj = db >> 1, half = db & 1;
        const int key0 = 16 * ks + 4 * h + q;
        const char* a0 = Vl + 384 * key0 + 128 * jj + 64 * (half ^ ((q >> 1) & 1)) + 32 * blk + 8 * p;
        typedef short v4i16_t __attribute__((ext_vector_type(4)));
        s16x4 lo = __builtin_bit_cast(s16x4, __builtin_amdgcn_ds_read_tr16_b64_v4i16((__attribute__((address_space(3))) v4i16_t*)a0));
        s16x4 hi = __builtin_bit_cast(s16x4, __builtin_amdgcn_ds_read_tr16_b64_v4i16((__attribute__((address_space(3))) v4i16_t*)(a0 + 384 * 8)));
        bf16x8 vf = __builtin_shufflevector(lo, hi, 0, 1, 2, 3, 4, 5, 6, 7);
        o[db] = __builtin_amdgcn_mfma_f32_32x32x16_bf16(vf, pf[ks], o[db], 0, 0, 0);
      }
  }
  l += __shfl_xor(l, 32);
  const float il = 1.f / l;
  uint16_t* Oh = O + ((size_t)head * SQ + qblk * 128 + w * 32 + r) * D;
#pragma unroll
  for (int db = 0; db < 6; ++db)
#pragma unroll
    for (int g = 0; g < 4; ++g) {
      u32x2 wv = {cvtpk(o[db][4 * g] * il, o[db][4 * g + 1] * il), cvtpk(o[db][4 * g + 2] * il, o[db][4 * g + 3] * il)};
      *(u32x2*)(Oh + 32 * db + 8 * g + 4 * h) = wv;
    }
}

extern "C" void kernel_launch(void* const* d_in, const int* in_sizes, int n_in, void* d_out, int out_size, void* d_ws, size_t ws_size,
                              hipStream_t stream) {
  attn_base<<<dim3(1024), dim3(256), 0, stream>>>((const uint16_t*)d_in[0], (const uint16_t*)d_in[1], (const uint16_t*)d_in[2], (uint16_t*)d_out);
}
```

```cpp
#include <hip/hip_runtime.h>
#include <stdint.h>

typedef unsigned u32x4 __attribute__((ext_vector_type(4)));

#define ATTN_BLKCHK 1
typedef short bf16x8 __attribute__((ext_vector_type(8)));
typedef float f32x4 __attribute__((ext_vector_type(4)));
typedef float f32x2 __attribute__((ext_vector_type(2)));
typedef __bf16 bf16x2 __attribute__((ext_vector_type(2)));
__device__ __forceinline__ unsigned pack_bf16(float lo, float hi) {
  f32x2 v = {lo, hi};
  return __builtin_bit_cast(unsigned, __builtin_convertvector(v, bf16x2));
}
__device__ __forceinline__ void attn_exact_rows(const char* Qr, const char* Kh, const char* Vh, char* Or, unsigned lane) {
  const unsigned r = lane & 15u, g = lane >> 4;
  const float c = 0.10411754627697264f;
  const bf16x8 ones = {0x3f80, 0x3f80, 0x3f80, 0x3f80, 0x3f80, 0x3f80, 0x3f80, 0x3f80};
#pragma unroll 1
  for (int qt = 0; qt < 4; ++qt) {
    bf16x8 qf[6];
#pragma unroll
    for (int ds = 0; ds < 6; ++ds) qf[ds] = *(const bf16x8*)(Qr + (16 * qt + r) * 384 + 64 * ds + 16 * g);
    f32x4 o[12], l = {0.f, 0.f, 0.f, 0.f};
#pragma unroll
    for (int dt = 0; dt < 12; ++dt) o[dt] = f32x4{0.f, 0.f, 0.f, 0.f};
    float m = -3.0e38f;
#pragma unroll 1
    for (int t = 0; t < 16; ++t) {
      f32x4 s[2];
#pragma unroll
      for (int kt = 0; kt < 2; ++kt) {
        s[kt] = f32x4{0.f, 0.f, 0.f, 0.f};
#pragma unroll
        for (int ds = 0; ds < 6; ++ds) {
          const bf16x8 kf = *(const bf16x8*)(Kh + (32 * t + 16 * kt + r) * 384 + 64 * ds + 16 * g);
          s[kt] = __builtin_amdgcn_mfma_f32_16x16x32_bf16(kf, qf[ds], s[kt], 0, 0, 0);
        }
      }
      float mx = fmaxf(fmaxf(fmaxf(s[0][0], s[0][1]), fmaxf(s[0][2], s[0][3])), fmaxf(fmaxf(s[1][0], s[1][1]), fmaxf(s[1][2], s[1][3])));
      mx = fmaxf(mx, __shfl_xor(mx, 16));
      mx = fmaxf(mx, __shfl_xor(mx, 32));
      const float mn = fmaxf(m, mx), f = __builtin_amdgcn_exp2f((m - mn) * c);
      m = mn;
      l *= f;
#pragma unroll
      for (int dt = 0; dt < 12; ++dt) o[dt] *= f;
      u32x4 pw;
#pragma unroll
      for (int kt = 0; kt < 2; ++kt) {
        pw[2 * kt] = pack_bf16(__builtin_amdgcn_exp2f((s[kt][0] - m) * c), __builtin_amdgcn_exp2f((s[kt][1] - m) * c));
        pw[2 * kt + 1] = pack_bf16(__builtin_amdgcn_exp2f((s[kt][2] - m) * c), __builtin_amdgcn_exp2f((s[kt][3] - m) * c));
      }
      const bf16x8 pf = __builtin_bit_cast(bf16x8, pw);
      l = __builtin_amdgcn_mfma_f32_16x16x32_bf16(ones, pf, l, 0, 0, 0);
#pragma unroll
      for (int dt = 0; dt < 12; ++dt) {
        bf16x8 vf;
#pragma unroll
        for (int j = 0; j < 8; ++j) vf[j] = *(const short*)(Vh + (32 * t + 16 * (j >> 2) + 4 * g + (j & 3)) * 384 + 2 * (16 * dt + r));
        o[dt] = __builtin_amdgcn_mfma_f32_16x16x32_bf16(vf, pf, o[dt], 0, 0, 0);
      }
    }
    const float inv = 1.f / l[0];
#pragma unroll
    for (int dt = 0; dt < 12; ++dt) {
      unsigned* dst = (unsigned*)(Or + (16 * qt + r) * 384 + 32 * dt + 8 * g);
      dst[0] = pack_bf16(o[dt][0] * inv, o[dt][1] * inv);
      dst[1] = pack_bf16(o[dt][2] * inv, o[dt][3] * inv);
    }
  }
}

extern "C" __global__ __launch_bounds__(256, 1) void attn_fwd_d192(const void* __restrict__ Qp, const void* __restrict__ Kp,
                                                                   const void* __restrict__ Vp, void* __restrict__ Op, void* __restrict__ Wp) {
  auto karg = __builtin_amdgcn_kernarg_segment_ptr();
  unsigned bid = blockIdx.x, tid = threadIdx.x;
  unsigned wave; unsigned long long bad0, bad1;
#define ATTN_EXTRA_OUT , "={s3}"(wave), "={s[96:97]}"(bad0), "={s[98:99]}"(bad1)
  asm volatile(R"ASM(
s_load_dwordx2 s[8:9], s[0:1], 0x8
s_load_dwordx2 s[16:17], s[0:1], 0x0
s_load_dwordx2 s[12:13], s[0:1], 0x10
s_load_dwordx2 s[20:21], s[0:1], 0x18
s_getpc_b64 s[36:37]
ANCH_%=:
s_lshr_b32 s40, s2, 3
s_lshl_b32 s40, s40, 12
v_lshl_add_u32 v1, v0, 4, s40
v_min_u32 v2, CEND_%= - ANCH_%= - 16, v1
global_load_dwordx4 a[96:99], v2, s[36:37]
v_lshrrev_b32 v1, 6, v0
v_and_b32 v0, 63, v0
v_readfirstlane_b32 s3, v1
s_and_b32 s40, s2, 7
s_lshr_b32 s41, s2, 4
s_lshl_b32 s40, s40, 4
s_add_u32 s40, s40, s41
s_mul_i32 s40, s40, 393216
s_add_u32 s10, s40, 393216
s_mov_b32 s14, s10
s_mov_b32 s11, 0x20000
s_mov_b32 s15, 0x20000
s_mov_b32 s19, 0x20000
s_mov_b32 s23, 0x20000
s_mov_b32 s18, 50331648
s_mov_b32 s22, 50331648
s_mul_i32 s28, s3, 3072
s_add_u32 s24, s40, s28
s_bfe_u32 s41, s2, 0x10003
s_mul_i32 s41, s41, 98304
s_mul_i32 s42, s3, 24576
s_add_u32 s26, s40, s41
s_add_u32 s26, s26, s42
s_lshl_b32 s41, s3, 14
s_add_u32 s41, s41, 114688
s_cmp_lt_u32 s3, 3
s_cselect_b32 s30, s41, 49152
s_and_b32 s31, s3, 1
s_lshl_b32 s31, s31, 6
v_lshrrev_b32 v1, 3, v0
v_and_b32 v2, 7, v0
v_lshlrev_b32 v3, 4, v2
v_lshl_or_b32 v3, v1, 7, v3
v_mad_u32_u24 v4, v1, 11, 0
v_lshrrev_b32 v4, 6, v4
v_lshlrev_b32 v5, 4, v4
v_xor_b32 v5, v5, v3
v_xor_b32 v234, s31, v5
v_lshlrev_b32 v4, 5, v4
v_xor_b32 v237, v4, v3
v_mul_u32_u24 v4, 11, v1
v_add_u32 v4, 88, v4
v_lshrrev_b32 v4, 6, v4
v_lshlrev_b32 v5, 4, v4
v_xor_b32 v5, v5, v3
v_xor_b32 v235, s31, v5
v_lshlrev_b32 v4, 5, v4
v_xor_b32 v238, v4, v3
v_mul_u32_u24 v4, 11, v1
v_add_u32 v4, 176, v4
v_lshrrev_b32 v4, 6, v4
v_lshlrev_b32 v5, 4, v4
v_xor_b32 v5, v5, v3
v_xor_b32 v236, s31, v5
v_lshlrev_b32 v4, 5, v4
v_xor_b32 v239, v4, v3
v_lshrrev_b32 v4, 4, v0
v_xor_b32 v5, v2, v4
v_lshlrev_b32 v5, 4, v5
v_mul_u32_u24 v6, 384, v1
v_add_u32 v250, v5, v6
v_xor_b32 v5, 64, v250
v_add_u32 v251, 3072, v5
v_and_b32 v7, 15, v0
v_mul_u32_u24 v8, 384, v7
v_lshl_add_u32 v254, v4, 4, v8
s_waitcnt lgkmcnt(0)
s_mov_b32 s25, s24
s_add_u32 s29, s28, 65536
s_add_u32 m0, s28, 0
s_nop 0
buffer_load_dwordx4 v234, s[8:11], s24 offen nt lds
buffer_load_dwordx4 v235, s[8:11], s24 offen offset:1024 nt lds
buffer_load_dwordx4 v236, s[8:11], s24 offen offset:2048 nt lds
s_add_u32 s24, s24, 12288
s_add_u32 s40, s26, 0
s_mov_b32 s41, s30
s_add_u32 m0, s41, 0
s_nop 0
buffer_load_dwordx4 v250, s[16:19], s40 offen nt lds
s_add_u32 m0, s41, 1024
s_nop 0
buffer_load_dwordx4 v251, s[16:19], s40 offen nt lds
s_add_u32 m0, s41, 1920
s_nop 0
buffer_load_dwordx4 v250, s[16:19], s40 offen offset:128 nt lds
s_add_u32 m0, s41, 2944
s_nop 0
buffer_load_dwordx4 v251, s[16:19], s40 offen offset:128 nt lds
s_add_u32 s40, s40, 6144
s_add_u32 s41, s41, 4096
s_add_u32 s42, s26, 0
buffer_load_dwordx4 a[16:19], v254, s[16:19], s42 offen offset:256 nt
buffer_load_dwordx4 a[20:23], v254, s[16:19], s42 offen offset:320 nt
s_add_u32 m0, s41, 0
s_nop 0
buffer_load_dwordx4 v250, s[16:19], s40 offen nt lds
s_add_u32 m0, s41, 1024
s_nop 0
buffer_load_dwordx4 v251, s[16:19], s40 offen nt lds
s_add_u32 m0, s41, 1920
s_nop 0
buffer_load_dwordx4 v250, s[16:19], s40 offen offset:128 nt lds
s_add_u32 m0, s41, 2944
s_nop 0
buffer_load_dwordx4 v251, s[16:19], s40 offen offset:128 nt lds
s_add_u32 s40, s40, 6144
s_add_u32 s41, s41, 4096
s_add_u32 s42, s26, 6144
buffer_load_dwordx4 a[40:43], v254, s[16:19], s42 offen offset:256 nt
buffer_load_dwordx4 a[44:47], v254, s[16:19], s42 offen offset:320 nt
v_lshrrev_b32 v5, 1, v7
v_xor_b32 v5, v5, v4
v_lshlrev_b32 v5, 4, v5
v_add_u32 v228, v5, v8
v_xor_b32 v6, 64, v5
v_add_u32 v229, v6, v8
v_lshlrev_b32 v9, 7, v7
v_add_u32 v9, s30, v9
v_add_u32 v252, v5, v9
v_add_u32 v253, v6, v9
v_lshrrev_b32 v5, 2, v0
v_mul_u32_u24 v5, 384, v5
v_and_b32 v6, 3, v0
v_lshl_add_u32 v5, v6, 3, v5
v_add_u32 v5, 0x10000, v5
v_and_b32 v6, 3, v1
v_xor_b32 v9, 0, v6
v_lshl_add_u32 v230, v9, 5, v5
v_xor_b32 v9, 1, v6
v_lshl_add_u32 v231, v9, 5, v5
v_xor_b32 v9, 2, v6
v_lshl_add_u32 v232, v9, 5, v5
v_xor_b32 v9, 3, v6
v_lshl_add_u32 v233, v9, 5, v5
s_mov_b32 s27, 0x3dd53b95
s_mov_b64 s[96:97], 0
s_mov_b64 s[98:99], 0
s_mov_b32 s50, 0
s_mov_b32 s48, s28
s_mov_b32 s51, 24576
s_add_u32 s49, s29, 24576
s_add_u32 s54, s30, 16384
s_mov_b32 s55, 0xc0000000
s_mov_b32 s43, 0x3f803f80
v_accvgpr_write_b32 a192, s43
v_accvgpr_write_b32 a193, s43
v_accvgpr_write_b32 a194, s43
v_accvgpr_write_b32 a195, s43
v_mov_b32 v96, 0
v_mov_b32 v97, 0
v_mov_b32 v98, 0
v_mov_b32 v99, 0
v_mov_b32 v100, 0
v_mov_b32 v101, 0
v_mov_b32 v102, 0
v_mov_b32 v103, 0
v_mov_b32 v104, 0
v_mov_b32 v105, 0
v_mov_b32 v106, 0
v_mov_b32 v107, 0
v_mov_b32 v108, 0
v_mov_b32 v109, 0
v_mov_b32 v110, 0
v_mov_b32 v111, 0
v_mov_b32 v112, 0
v_mov_b32 v113, 0
v_mov_b32 v114, 0
v_mov_b32 v115, 0
v_mov_b32 v116, 0
v_mov_b32 v117, 0
v_mov_b32 v118, 0
v_mov_b32 v119, 0
v_mov_b32 v120, 0
v_mov_b32 v121, 0
v_mov_b32 v122, 0
v_mov_b32 v123, 0
v_mov_b32 v124, 0
v_mov_b32 v125, 0
v_mov_b32 v126, 0
v_mov_b32 v127, 0
v_mov_b32 v128, 0
v_mov_b32 v129, 0
v_mov_b32 v130, 0
v_mov_b32 v131, 0
v_mov_b32 v132, 0
v_mov_b32 v133, 0
v_mov_b32 v134, 0
v_mov_b32 v135, 0
v_mov_b32 v136, 0
v_mov_b32 v137, 0
v_mov_b32 v138, 0
v_mov_b32 v139, 0
v_mov_b32 v140, 0
v_mov_b32 v141, 0
v_mov_b32 v142, 0
v_mov_b32 v143, 0
v_mov_b32 v144, 0
v_mov_b32 v145, 0
v_mov_b32 v146, 0
v_mov_b32 v147, 0
v_mov_b32 v148, 0
v_mov_b32 v149, 0
v_mov_b32 v150, 0
v_mov_b32 v151, 0
v_mov_b32 v152, 0
v_mov_b32 v153, 0
v_mov_b32 v154, 0
v_mov_b32 v155, 0
v_mov_b32 v156, 0
v_mov_b32 v157, 0
v_mov_b32 v158, 0
v_mov_b32 v159, 0
v_mov_b32 v160, 0
v_mov_b32 v161, 0
v_mov_b32 v162, 0
v_mov_b32 v163, 0
v_mov_b32 v164, 0
v_mov_b32 v165, 0
v_mov_b32 v166, 0
v_mov_b32 v167, 0
v_mov_b32 v168, 0
v_mov_b32 v169, 0
v_mov_b32 v170, 0
v_mov_b32 v171, 0
v_mov_b32 v172, 0
v_mov_b32 v173, 0
v_mov_b32 v174, 0
v_mov_b32 v175, 0
v_mov_b32 v176, 0
v_mov_b32 v177, 0
v_mov_b32 v178, 0
v_mov_b32 v179, 0
v_mov_b32 v180, 0
v_mov_b32 v181, 0
v_mov_b32 v182, 0
v_mov_b32 v183, 0
v_mov_b32 v184, 0
v_mov_b32 v185, 0
v_mov_b32 v186, 0
v_mov_b32 v187, 0
v_mov_b32 v188, 0
v_mov_b32 v189, 0
v_mov_b32 v190, 0
v_mov_b32 v191, 0
v_mov_b32 v192, 0
v_mov_b32 v193, 0
v_mov_b32 v194, 0
v_mov_b32 v195, 0
v_mov_b32 v196, 0
v_mov_b32 v197, 0
v_mov_b32 v198, 0
v_mov_b32 v199, 0
v_mov_b32 v200, 0
v_mov_b32 v201, 0
v_mov_b32 v202, 0
v_mov_b32 v203, 0
v_mov_b32 v204, 0
v_mov_b32 v205, 0
v_mov_b32 v206, 0
v_mov_b32 v207, 0
v_mov_b32 v208, 0
v_mov_b32 v209, 0
v_mov_b32 v210, 0
v_mov_b32 v211, 0
v_mov_b32 v212, 0
v_mov_b32 v213, 0
v_mov_b32 v214, 0
v_mov_b32 v215, 0
v_mov_b32 v216, 0
v_mov_b32 v217, 0
v_mov_b32 v218, 0
v_mov_b32 v219, 0
v_mov_b32 v220, 0
v_mov_b32 v221, 0
v_mov_b32 v222, 0
v_mov_b32 v223, 0
v_mov_b32 v224, 0
v_mov_b32 v225, 0
v_mov_b32 v226, 0
v_mov_b32 v227, 0
v_accvgpr_write_b32 a196, 0
v_accvgpr_write_b32 a197, 0
v_accvgpr_write_b32 a198, 0
v_accvgpr_write_b32 a199, 0
v_accvgpr_write_b32 a200, 0
v_accvgpr_write_b32 a201, 0
v_accvgpr_write_b32 a202, 0
v_accvgpr_write_b32 a203, 0
v_accvgpr_write_b32 a204, 0
v_accvgpr_write_b32 a205, 0
v_accvgpr_write_b32 a206, 0
v_accvgpr_write_b32 a207, 0
v_accvgpr_write_b32 a208, 0
v_accvgpr_write_b32 a209, 0
v_accvgpr_write_b32 a210, 0
v_accvgpr_write_b32 a211, 0
v_accvgpr_write_b32 a212, 0
v_accvgpr_write_b32 a213, 0
v_accvgpr_write_b32 a214, 0
v_accvgpr_write_b32 a215, 0
v_accvgpr_write_b32 a216, 0
v_accvgpr_write_b32 a217, 0
v_accvgpr_write_b32 a218, 0
v_accvgpr_write_b32 a219, 0
v_accvgpr_write_b32 a220, 0
v_accvgpr_write_b32 a221, 0
v_accvgpr_write_b32 a222, 0
v_accvgpr_write_b32 a223, 0
v_accvgpr_write_b32 a224, 0
v_accvgpr_write_b32 a225, 0
v_accvgpr_write_b32 a226, 0
v_accvgpr_write_b32 a227, 0
v_accvgpr_write_b32 a228, 0
v_accvgpr_write_b32 a229, 0
v_accvgpr_write_b32 a230, 0
v_accvgpr_write_b32 a231, 0
v_accvgpr_write_b32 a232, 0
v_accvgpr_write_b32 a233, 0
v_accvgpr_write_b32 a234, 0
v_accvgpr_write_b32 a235, 0
v_accvgpr_write_b32 a236, 0
v_accvgpr_write_b32 a237, 0
v_accvgpr_write_b32 a238, 0
v_accvgpr_write_b32 a239, 0
v_accvgpr_write_b32 a240, 0
v_accvgpr_write_b32 a241, 0
v_accvgpr_write_b32 a242, 0
v_accvgpr_write_b32 a243, 0
v_accvgpr_write_b32 a244, 0
v_accvgpr_write_b32 a245, 0
v_accvgpr_write_b32 a246, 0
v_accvgpr_write_b32 a247, 0
v_accvgpr_write_b32 a248, 0
v_accvgpr_write_b32 a249, 0
v_accvgpr_write_b32 a250, 0
v_accvgpr_write_b32 a251, 0
v_accvgpr_write_b32 a252, 0
v_accvgpr_write_b32 a253, 0
v_accvgpr_write_b32 a254, 0
v_accvgpr_write_b32 a255, 0
v_accvgpr_write_b32 a176, 0
v_accvgpr_write_b32 a177, 0
v_accvgpr_write_b32 a178, 0
v_accvgpr_write_b32 a179, 0
v_accvgpr_write_b32 a180, 0
v_accvgpr_write_b32 a181, 0
v_accvgpr_write_b32 a182, 0
v_accvgpr_write_b32 a183, 0
v_accvgpr_write_b32 a184, 0
v_accvgpr_write_b32 a185, 0
v_accvgpr_write_b32 a186, 0
v_accvgpr_write_b32 a187, 0
v_accvgpr_write_b32 a188, 0
v_accvgpr_write_b32 a189, 0
v_accvgpr_write_b32 a190, 0
v_accvgpr_write_b32 a191, 0
s_waitcnt vmcnt(12)
s_barrier
ds_read_b128 a[128:131], v228 offset:0
ds_read_b128 a[132:135], v229 offset:0
ds_read_b128 a[136:139], v228 offset:128
ds_read_b128 a[140:143], v229 offset:128
ds_read_b128 a[144:147], v228 offset:256
ds_read_b128 a[148:151], v229 offset:256
ds_read_b128 a[152:155], v228 offset:6144
ds_read_b128 a[156:159], v229 offset:6144
ds_read_b128 a[160:163], v228 offset:6272
ds_read_b128 a[164:167], v229 offset:6272
ds_read_b128 a[168:171], v228 offset:6400
ds_read_b128 a[172:175], v229 offset:6400
s_waitcnt vmcnt(8)
ds_read_b128 a[0:3], v252 offset:0
ds_read_b128 a[4:7], v253 offset:0
ds_read_b128 a[8:11], v252 offset:2048
ds_read_b128 a[12:15], v253 offset:2048
s_add_u32 m0, s41, 0
s_nop 0
buffer_load_dwordx4 v250, s[16:19], s40 offen nt lds
s_add_u32 m0, s41, 1024
s_nop 0
buffer_load_dwordx4 v251, s[16:19], s40 offen nt lds
s_add_u32 m0, s41, 1920
s_nop 0
buffer_load_dwordx4 v250, s[16:19], s40 offen offset:128 nt lds
s_add_u32 m0, s41, 2944
s_nop 0
buffer_load_dwordx4 v251, s[16:19], s40 offen offset:128 nt lds
s_add_u32 s40, s40, 6144
s_add_u32 s41, s41, 4096
s_add_u32 s42, s26, 12288
buffer_load_dwordx4 a[64:67], v254, s[16:19], s42 offen offset:256 nt
buffer_load_dwordx4 a[68:71], v254, s[16:19], s42 offen offset:320 nt
s_add_u32 m0, s28, 12288
s_nop 0
buffer_load_dwordx4 v234, s[8:11], s24 offen nt lds
buffer_load_dwordx4 v235, s[8:11], s24 offen offset:1024 nt lds
buffer_load_dwordx4 v236, s[8:11], s24 offen offset:2048 nt lds
s_add_u32 s24, s24, 12288
s_waitcnt lgkmcnt(0)
s_waitcnt vmcnt(15)
v_mfma_f32_16x16x32_bf16 v[0:3], a[128:131], a[0:3], 0
v_mfma_f32_16x16x32_bf16 v[0:3], a[132:135], a[4:7], v[0:3]
v_mfma_f32_16x16x32_bf16 v[0:3], a[136:139], a[8:11], v[0:3]
v_mfma_f32_16x16x32_bf16 v[0:3], a[140:143], a[12:15], v[0:3]
v_mfma_f32_16x16x32_bf16 v[0:3], a[144:147], a[16:19], v[0:3]
v_mfma_f32_16x16x32_bf16 v[0:3], a[148:151], a[20:23], v[0:3]
v_mfma_f32_16x16x32_bf16 v[16:19], a[152:155], a[0:3], 0
v_mfma_f32_16x16x32_bf16 v[16:19], a[156:159], a[4:7], v[16:19]
v_mfma_f32_16x16x32_bf16 v[16:19], a[160:163], a[8:11], v[16:19]
v_mfma_f32_16x16x32_bf16 v[16:19], a[164:167], a[12:15], v[16:19]
v_mfma_f32_16x16x32_bf16 v[16:19], a[168:171], a[16:19], v[16:19]
v_mfma_f32_16x16x32_bf16 v[16:19], a[172:175], a[20:23], v[16:19]
s_nop 7
s_mov_b32 s42, 0xc1c00000
v_max3_f32 v244, v0, v1, v2
v_max3_f32 v245, v3, v16, v17
v_max3_f32 v244, v244, v18, v19
v_max_f32 v244, v244, v245
v_mov_b32 v245, v244
s_nop 1
v_permlane32_swap_b32 v244, v245
v_max_f32 v244, v244, v245
v_mov_b32 v245, v244
s_nop 1
v_permlane16_swap_b32 v244, v245
v_max_f32 v244, v244, v245
v_mov_b32 v245, s42
v_fma_f32 v240, -v244, s27, v245
v_fma_f32 v244, v0, s27, v240
v_fma_f32 v245, v1, s27, v240
v_exp_f32 v244, v244
v_exp_f32 v245, v245
s_nop 0
v_cvt_pk_bf16_f32 v64, v244, v245
v_fma_f32 v246, v2, s27, v240
v_fma_f32 v247, v3, s27, v240
v_exp_f32 v246, v246
v_exp_f32 v247, v247
s_nop 0
v_cvt_pk_bf16_f32 v65, v246, v247
v_fma_f32 v244, v16, s27, v240
v_fma_f32 v245, v17, s27, v240
v_exp_f32 v244, v244
v_exp_f32 v245, v245
s_nop 0
v_cvt_pk_bf16_f32 v66, v244, v245
v_fma_f32 v246, v18, s27, v240
v_fma_f32 v247, v19, s27, v240
v_exp_f32 v246, v246
v_exp_f32 v247, v247
s_nop 0
v_cvt_pk_bf16_f32 v67, v246, v247
s_waitcnt vmcnt(11)
ds_read_b128 a[24:27], v252 offset:4096
ds_read_b128 a[28:31], v253 offset:4096
ds_read_b128 a[32:35], v252 offset:6144
ds_read_b128 a[36:39], v253 offset:6144
s_add_u32 m0, s29, 0
s_nop 0
buffer_load_dwordx4 v237, s[12:15], s25 offen nt lds
buffer_load_dwordx4 v238, s[12:15], s25 offen offset:1024 nt lds
buffer_load_dwordx4 v239, s[12:15], s25 offen offset:2048 nt lds
s_add_u32 s25, s25, 12288
s_add_u32 m0, s28, 24576
s_nop 0
buffer_load_dwordx4 v234, s[8:11], s24 offen nt lds
buffer_load_dwordx4 v235, s[8:11], s24 offen offset:1024 nt lds
buffer_load_dwordx4 v236, s[8:11], s24 offen offset:2048 nt lds
s_add_u32 s24, s24, 12288
s_add_u32 m0, s41, 0
s_nop 0
buffer_load_dwordx4 v250, s[16:19], s40 offen nt lds
s_add_u32 m0, s41, 1024
s_nop 0
buffer_load_dwordx4 v251, s[16:19], s40 offen nt lds
s_add_u32 m0, s41, 1920
s_nop 0
buffer_load_dwordx4 v250, s[16:19], s40 offen offset:128 nt lds
s_add_u32 m0, s41, 2944
s_nop 0
buffer_load_dwordx4 v251, s[16:19], s40 offen offset:128 nt lds
s_add_u32 s40, s40, 6144
s_add_u32 s41, s41, 4096
s_add_u32 s42, s26, 18432
buffer_load_dwordx4 a[88:91], v254, s[16:19], s42 offen offset:256 nt
buffer_load_dwordx4 a[92:95], v254, s[16:19], s42 offen offset:320 nt
s_waitcnt lgkmcnt(0)
s_waitcnt vmcnt(21)
v_mfma_f32_16x16x32_bf16 v[4:7], a[128:131], a[24:27], 0
v_mfma_f32_16x16x32_bf16 v[4:7], a[132:135], a[28:31], v[4:7]
v_mfma_f32_16x16x32_bf16 v[4:7], a[136:139], a[32:35], v[4:7]
v_mfma_f32_16x16x32_bf16 v[4:7], a[140:143], a[36:39], v[4:7]
v_mfma_f32_16x16x32_bf16 v[4:7], a[144:147], a[40:43], v[4:7]
v_mfma_f32_16x16x32_bf16 v[4:7], a[148:151], a[44:47], v[4:7]
v_mfma_f32_16x16x32_bf16 v[20:23], a[152:155], a[24:27], 0
v_mfma_f32_16x16x32_bf16 v[20:23], a[156:159], a[28:31], v[20:23]
v_mfma_f32_16x16x32_bf16 v[20:23], a[160:163], a[32:35], v[20:23]
v_mfma_f32_16x16x32_bf16 v[20:23], a[164:167], a[36:39], v[20:23]
v_mfma_f32_16x16x32_bf16 v[20:23], a[168:171], a[40:43], v[20:23]
v_mfma_f32_16x16x32_bf16 v[20:23], a[172:175], a[44:47], v[20:23]
s_nop 7
s_mov_b32 s42, 0xc1c00000
v_max3_f32 v244, v4, v5, v6
v_max3_f32 v245, v7, v20, v21
v_max3_f32 v244, v244, v22, v23
v_max_f32 v244, v244, v245
v_mov_b32 v245, v244
s_nop 1
v_permlane32_swap_b32 v244, v245
v_max_f32 v244, v244, v245
v_mov_b32 v245, v244
s_nop 1
v_permlane16_swap_b32 v244, v245
v_max_f32 v244, v244, v245
v_mov_b32 v245, s42
v_fma_f32 v241, -v244, s27, v245
v_fma_f32 v244, v4, s27, v241
v_fma_f32 v245, v5, s27, v241
v_exp_f32 v244, v244
v_exp_f32 v245, v245
s_nop 0
v_cvt_pk_bf16_f32 v68, v244, v245
v_fma_f32 v246, v6, s27, v241
v_fma_f32 v247, v7, s27, v241
v_exp_f32 v246, v246
v_exp_f32 v247, v247
s_nop 0
v_cvt_pk_bf16_f32 v69, v246, v247
v_fma_f32 v244, v20, s27, v241
v_fma_f32 v245, v21, s27, v241
v_exp_f32 v244, v244
v_exp_f32 v245, v245
s_nop 0
v_cvt_pk_bf16_f32 v70, v244, v245
v_fma_f32 v246, v22, s27, v241
v_fma_f32 v247, v23, s27, v241
v_exp_f32 v246, v246
v_exp_f32 v247, v247
s_nop 0
v_cvt_pk_bf16_f32 v71, v246, v247
s_waitcnt vmcnt(17)
ds_read_b128 a[48:51], v252 offset:8192
ds_read_b128 a[52:55], v253 offset:8192
ds_read_b128 a[56:59], v252 offset:10240
ds_read_b128 a[60:63], v253 offset:10240
s_add_u32 m0, s29, 12288
s_nop 0
buffer_load_dwordx4 v237, s[12:15], s25 offen nt lds
buffer_load_dwordx4 v238, s[12:15], s25 offen offset:1024 nt lds
buffer_load_dwordx4 v239, s[12:15], s25 offen offset:2048 nt lds
s_add_u32 s25, s25, 12288
s_add_u32 m0, s28, 36864
s_nop 0
buffer_load_dwordx4 v234, s[8:11], s24 offen nt lds
buffer_load_dwordx4 v235, s[8:11], s24 offen offset:1024 nt lds
buffer_load_dwordx4 v236, s[8:11], s24 offen offset:2048 nt lds
s_add_u32 s24, s24, 12288
s_waitcnt lgkmcnt(0)
s_waitcnt vmcnt(21)
v_mfma_f32_16x16x32_bf16 v[8:11], a[128:131], a[48:51], 0
v_mfma_f32_16x16x32_bf16 v[8:11], a[132:135], a[52:55], v[8:11]
v_mfma_f32_16x16x32_bf16 v[8:11], a[136:139], a[56:59], v[8:11]
v_mfma_f32_16x16x32_bf16 v[8:11], a[140:143], a[60:63], v[8:11]
v_mfma_f32_16x16x32_bf16 v[8:11], a[144:147], a[64:67], v[8:11]
v_mfma_f32_16x16x32_bf16 v[8:11], a[148:151], a[68:71], v[8:11]
v_mfma_f32_16x16x32_bf16 v[24:27], a[152:155], a[48:51], 0
v_mfma_f32_16x16x32_bf16 v[24:27], a[156:159], a[52:55], v[24:27]
v_mfma_f32_16x16x32_bf16 v[24:27], a[160:163], a[56:59], v[24:27]
v_mfma_f32_16x16x32_bf16 v[24:27], a[164:167], a[60:63], v[24:27]
v_mfma_f32_16x16x32_bf16 v[24:27], a[168:171], a[64:67], v[24:27]
v_mfma_f32_16x16x32_bf16 v[24:27], a[172:175], a[68:71], v[24:27]
s_nop 7
s_mov_b32 s42, 0xc1c00000
v_max3_f32 v244, v8, v9, v10
v_max3_f32 v245, v11, v24, v25
v_max3_f32 v244, v244, v26, v27
v_max_f32 v244, v244, v245
v_mov_b32 v245, v244
s_nop 1
v_permlane32_swap_b32 v244, v245
v_max_f32 v244, v244, v245
v_mov_b32 v245, v244
s_nop 1
v_permlane16_swap_b32 v244, v245
v_max_f32 v244, v244, v245
v_mov_b32 v245, s42
v_fma_f32 v242, -v244, s27, v245
v_fma_f32 v244, v8, s27, v242
v_fma_f32 v245, v9, s27, v242
v_exp_f32 v244, v244
v_exp_f32 v245, v245
s_nop 0
v_cvt_pk_bf16_f32 v72, v244, v245
v_fma_f32 v246, v10, s27, v242
v_fma_f32 v247, v11, s27, v242
v_exp_f32 v246, v246
v_exp_f32 v247, v247
s_nop 0
v_cvt_pk_bf16_f32 v73, v246, v247
v_fma_f32 v244, v24, s27, v242
v_fma_f32 v245, v25, s27, v242
v_exp_f32 v244, v244
v_exp_f32 v245, v245
s_nop 0
v_cvt_pk_bf16_f32 v74, v244, v245
v_fma_f32 v246, v26, s27, v242
v_fma_f32 v247, v27, s27, v242
v_exp_f32 v246, v246
v_exp_f32 v247, v247
s_nop 0
v_cvt_pk_bf16_f32 v75, v246, v247
s_waitcnt vmcnt(8)
ds_read_b128 a[72:75], v252 offset:12288
ds_read_b128 a[76:79], v253 offset:12288
ds_read_b128 a[80:83], v252 offset:14336
ds_read_b128 a[84:87], v253 offset:14336
s_waitcnt vmcnt(6)
s_barrier
s_add_u32 m0, s29, 24576
s_nop 0
buffer_load_dwordx4 v237, s[12:15], s25 offen nt lds
buffer_load_dwordx4 v238, s[12:15], s25 offen offset:1024 nt lds
buffer_load_dwordx4 v239, s[12:15], s25 offen offset:2048 nt lds
s_add_u32 s25, s25, 12288
s_add_u32 s40, s26, 196608
s_mov_b32 s41, s30
s_waitcnt lgkmcnt(0)
v_mfma_f32_16x16x32_bf16 v[12:15], a[128:131], a[72:75], 0
v_mfma_f32_16x16x32_bf16 v[12:15], a[132:135], a[76:79], v[12:15]
v_mfma_f32_16x16x32_bf16 v[12:15], a[136:139], a[80:83], v[12:15]
v_mfma_f32_16x16x32_bf16 v[12:15], a[140:143], a[84:87], v[12:15]
v_mfma_f32_16x16x32_bf16 v[12:15], a[144:147], a[88:91], v[12:15]
v_mfma_f32_16x16x32_bf16 v[12:15], a[148:151], a[92:95], v[12:15]
v_mfma_f32_16x16x32_bf16 v[28:31], a[152:155], a[72:75], 0
ds_read_b128 a[128:131], v228 offset:12288
v_mfma_f32_16x16x32_bf16 v[28:31], a[156:159], a[76:79], v[28:31]
ds_read_b128 a[132:135], v229 offset:12288
v_mfma_f32_16x16x32_bf16 v[28:31], a[160:163], a[80:83], v[28:31]
ds_read_b128 a[136:139], v228 offset:12416
v_mfma_f32_16x16x32_bf16 v[28:31], a[164:167], a[84:87], v[28:31]
ds_read_b128 a[140:143], v229 offset:12416
v_mfma_f32_16x16x32_bf16 v[28:31], a[168:171], a[88:91], v[28:31]
ds_read_b128 a[144:147], v228 offset:12544
v_mfma_f32_16x16x32_bf16 v[28:31], a[172:175], a[92:95], v[28:31]
ds_read_b128 a[148:151], v229 offset:12544
; ==== period 0
s_waitcnt lgkmcnt(0)
v_mfma_f32_16x16x32_bf16 v[32:35], a[128:131], a[0:3], 0
ds_read_b128 a[152:155], v228 offset:18432
v_mfma_f32_16x16x32_bf16 v[32:35], a[132:135], a[4:7], v[32:35]
v_mfma_f32_16x16x32_bf16 v[32:35], a[136:139], a[8:11], v[32:35]
ds_read_b128 a[156:159], v229 offset:18432
v_mfma_f32_16x16x32_bf16 v[32:35], a[140:143], a[12:15], v[32:35]
s_mov_b32 s42, 0xc1c00000
v_max3_f32 v244, v12, v13, v14
v_max3_f32 v245, v15, v28, v29
v_mfma_f32_16x16x32_bf16 v[32:35], a[144:147], a[16:19], v[32:35]
ds_read_b128 a[160:163], v228 offset:18560
v_max3_f32 v244, v244, v30, v31
v_mfma_f32_16x16x32_bf16 v[32:35], a[148:151], a[20:23], v[32:35]
v_max_f32 v244, v244, v245
v_mfma_f32_16x16x32_bf16 v[36:39], a[128:131], a[24:27], 0
ds_read_b128 a[164:167], v229 offset:18560
v_mov_b32 v245, v244
v_mfma_f32_16x16x32_bf16 v[36:39], a[132:135], a[28:31], v[36:39]
v_mfma_f32_16x16x32_bf16 v[36:39], a[136:139], a[32:35], v[36:39]
ds_read_b128 a[168:171], v228 offset:18688
v_permlane32_swap_b32 v244, v245
v_max_f32 v244, v244, v245
v_mfma_f32_16x16x32_bf16 v[36:39], a[140:143], a[36:39], v[36:39]
v_mov_b32 v245, v244
v_mfma_f32_16x16x32_bf16 v[36:39], a[144:147], a[40:43], v[36:39]
v_mfma_f32_16x16x32_bf16 v[36:39], a[148:151], a[44:47], v[36:39]
ds_read_b128 a[172:175], v229 offset:18688
v_permlane16_swap_b32 v244, v245
v_max_f32 v244, v244, v245
v_mfma_f32_16x16x32_bf16 v[40:43], a[128:131], a[48:51], 0
s_add_u32 m0, s48, 0
v_mov_b32 v245, s42
v_fma_f32 v243, -v244, s27, v245
v_mfma_f32_16x16x32_bf16 v[40:43], a[132:135], a[52:55], v[40:43]
v_xor_b32 v228, 24576, v228
buffer_load_dwordx4 v234, s[8:11], s24 offen nt lds
v_fma_f32 v244, v12, s27, v243
v_mfma_f32_16x16x32_bf16 v[40:43], a[136:139], a[56:59], v[40:43]
buffer_load_dwordx4 v235, s[8:11], s24 offen offset:1024 nt lds
v_fma_f32 v245, v13, s27, v243
v_mfma_f32_16x16x32_bf16 v[40:43], a[140:143], a[60:63], v[40:43]
v_xor_b32 v229, 24576, v229
buffer_load_dwordx4 v236, s[8:11], s24 offen offset:2048 nt lds
v_mfma_f32_16x16x32_bf16 v[40:43], a[144:147], a[64:67], v[40:43]
s_add_u32 s24, s24, 12288
v_exp_f32 v244, v244
v_mfma_f32_16x16x32_bf16 v[40:43], a[148:151], a[68:71], v[40:43]
s_add_u32 m0, s49, 12288
v_mfma_f32_16x16x32_bf16 v[44:47], a[128:131], a[72:75], 0
buffer_load_dwordx4 v237, s[12:15], s25 offen nt lds
v_exp_f32 v245, v245
v_mfma_f32_16x16x32_bf16 v[44:47], a[132:135], a[76:79], v[44:47]
buffer_load_dwordx4 v238, s[12:15], s25 offen offset:1024 nt lds
v_mfma_f32_16x16x32_bf16 v[44:47], a[136:139], a[80:83], v[44:47]
buffer_load_dwordx4 v239, s[12:15], s25 offen offset:2048 nt lds
v_cvt_pk_bf16_f32 v76, v244, v245
v_mfma_f32_16x16x32_bf16 v[44:47], a[140:143], a[84:87], v[44:47]
s_add_u32 s25, s25, 12288
v_fma_f32 v246, v14, s27, v243
v_mfma_f32_16x16x32_bf16 v[44:47], a[144:147], a[88:91], v[44:47]
s_xor_b32 s51, s51, 24576
v_fma_f32 v247, v15, s27, v243
v_mfma_f32_16x16x32_bf16 v[44:47], a[148:151], a[92:95], v[44:47]
s_add_u32 s49, s29, s51
v_exp_f32 v246, v246
s_waitcnt lgkmcnt(0)
v_mfma_f32_16x16x32_bf16 v[48:51], a[152:155], a[0:3], 0
ds_read_b64_tr_b16 a[128:129], v230 offset:0
v_mfma_f32_16x16x32_bf16 v[48:51], a[156:159], a[4:7], v[48:51]
ds_read_b64_tr_b16 a[130:131], v230 offset:6144
v_exp_f32 v247, v247
v_mfma_f32_16x16x32_bf16 v[48:51], a[160:163], a[8:11], v[48:51]
ds_read_b64_tr_b16 a[132:133], v231 offset:0
v_mfma_f32_16x16x32_bf16 v[48:51], a[164:167], a[12:15], v[48:51]
v_cvt_pk_bf16_f32 v77, v246, v247
v_mfma_f32_16x16x32_bf16 v[48:51], a[168:171], a[16:19], v[48:51]
ds_read_b64_tr_b16 a[134:135], v231 offset:6144
v_fma_f32 v244, v28, s27, v243
v_mfma_f32_16x16x32_bf16 v[48:51], a[172:175], a[20:23], v[48:51]
ds_read_b64_tr_b16 a[136:137], v232 offset:0
v_fma_f32 v245, v29, s27, v243
v_mfma_f32_16x16x32_bf16 v[52:55], a[152:155], a[24:27], 0
ds_read_b64_tr_b16 a[138:139], v232 offset:6144
v_exp_f32 v244, v244
v_mfma_f32_16x16x32_bf16 v[52:55], a[156:159], a[28:31], v[52:55]
v_mfma_f32_16x16x32_bf16 v[52:55], a[160:163], a[32:35], v[52:55]
ds_read_b64_tr_b16 a[140:141], v233 offset:0
v_exp_f32 v245, v245
v_mfma_f32_16x16x32_bf16 v[52:55], a[164:167], a[36:39], v[52:55]
ds_read_b64_tr_b16 a[142:143], v233 offset:6144
v_mfma_f32_16x16x32_bf16 v[52:55], a[168:171], a[40:43], v[52:55]
ds_read_b64_tr_b16 a[144:145], v230 offset:128
v_cvt_pk_bf16_f32 v78, v244, v245
v_mfma_f32_16x16x32_bf16 v[52:55], a[172:175], a[44:47], v[52:55]
v_fma_f32 v246, v30, s27, v243
v_mfma_f32_16x16x32_bf16 v[56:59], a[152:155], a[48:51], 0
ds_read_b64_tr_b16 a[146:147], v230 offset:6272
s_add_u32 s42, s26, 196608
v_fma_f32 v247, v31, s27, v243
v_mfma_f32_16x16x32_bf16 v[56:59], a[156:159], a[52:55], v[56:59]
ds_read_b64_tr_b16 a[148:149], v231 offset:128
buffer_load_dwordx4 a[96:99], v254, s[16:19], s42 offen offset:256 nt
v_mfma_f32_16x16x32_bf16 v[56:59], a[160:163], a[56:59], v[56:59]
buffer_load_dwordx4 a[100:103], v254, s[16:19], s42 offen offset:320 nt
v_exp_f32 v246, v246
v_mfma_f32_16x16x32_bf16 v[56:59], a[164:167], a[60:63], v[56:59]
ds_read_b64_tr_b16 a[150:151], v231 offset:6272
s_add_u32 s42, s26, 202752
v_mfma_f32_16x16x32_bf16 v[56:59], a[168:171], a[64:67], v[56:59]
buffer_load_dwordx4 a[104:107], v254, s[16:19], s42 offen offset:256 nt
v_exp_f32 v247, v247
v_mfma_f32_16x16x32_bf16 v[56:59], a[172:175], a[68:71], v[56:59]
buffer_load_dwordx4 a[108:111], v254, s[16:19], s42 offen offset:320 nt
v_mfma_f32_16x16x32_bf16 v[60:63], a[152:155], a[72:75], 0
s_add_u32 s42, s26, 208896
v_cvt_pk_bf16_f32 v79, v246, v247
v_mfma_f32_16x16x32_bf16 v[60:63], a[156:159], a[76:79], v[60:63]
buffer_load_dwordx4 a[112:115], v254, s[16:19], s42 offen offset:256 nt
v_mfma_f32_16x16x32_bf16 v[60:63], a[160:163], a[80:83], v[60:63]
buffer_load_dwordx4 a[116:119], v254, s[16:19], s42 offen offset:320 nt
v_mfma_f32_16x16x32_bf16 v[60:63], a[164:167], a[84:87], v[60:63]
s_add_u32 s42, s26, 215040
v_mfma_f32_16x16x32_bf16 v[60:63], a[168:171], a[88:91], v[60:63]
buffer_load_dwordx4 a[120:123], v254, s[16:19], s42 offen offset:256 nt
v_mfma_f32_16x16x32_bf16 v[60:63], a[172:175], a[92:95], v[60:63]
buffer_load_dwordx4 a[124:127], v254, s[16:19], s42 offen offset:320 nt
s_mov_b32 s52, 7
LOOP1_%=:
; ==== period L1 parity 1
s_waitcnt lgkmcnt(0)
v_mfma_f32_16x16x32_bf16 a[176:179], a[192:195], v[64:67], a[176:179]
ds_read_b64_tr_b16 a[152:153], v232 offset:128
v_fma_f32 v244, v32, s27, v240
v_mfma_f32_16x16x32_bf16 a[180:183], a[192:195], v[68:71], a[180:183]
ds_read_b64_tr_b16 a[154:155], v232 offset:6272
v_fma_f32 v245, v33, s27, v240
v_mfma_f32_16x16x32_bf16 a[184:187], a[192:195], v[72:75], a[184:187]
ds_read_b64_tr_b16 a[156:157], v233 offset:128
v_fma_f32 v246, v34, s27, v240
v_mfma_f32_16x16x32_bf16 a[188:191], a[192:195], v[76:79], a[188:191]
v_exp_f32 v244, v244
v_mfma_f32_16x16x32_bf16 v[96:99], a[128:131], v[64:67], v[96:99]
ds_read_b64_tr_b16 a[158:159], v233 offset:6272
v_fma_f32 v247, v35, s27, v240
v_mfma_f32_16x16x32_bf16 v[100:103], a[128:131], v[68:71], v[100:103]
ds_read_b64_tr_b16 a[160:161], v230 offset:256
v_mfma_f32_16x16x32_bf16 v[104:107], a[128:131], v[72:75], v[104:107]
ds_read_b64_tr_b16 a[162:163], v230 offset:6400
v_mfma_f32_16x16x32_bf16 v[108:111], a[128:131], v[76:79], v[108:111]
v_exp_f32 v245, v245
v_mfma_f32_16x16x32_bf16 v[112:115], a[132:135], v[64:67], v[112:115]
ds_read_b64_tr_b16 a[164:165], v231 offset:256
v_cvt_pk_bf16_f32 v80, v244, v245
v_mfma_f32_16x16x32_bf16 v[116:119], a[132:135], v[68:71], v[116:119]
ds_read_b64_tr_b16 a[166:167], v231 offset:6400
v_fma_f32 v244, v36, s27, v241
v_mfma_f32_16x16x32_bf16 v[120:123], a[132:135], v[72:75], v[120:123]
ds_read_b64_tr_b16 a[168:169], v232 offset:256
v_fma_f32 v245, v37, s27, v241
v_mfma_f32_16x16x32_bf16 v[124:127], a[132:135], v[76:79], v[124:127]
v_exp_f32 v246, v246
v_mfma_f32_16x16x32_bf16 v[128:131], a[136:139], v[64:67], v[128:131]
ds_read_b64_tr_b16 a[170:171], v232 offset:6400
v_mfma_f32_16x16x32_bf16 v[132:135], a[136:139], v[68:71], v[132:135]
ds_read_b64_tr_b16 a[172:173], v233 offset:256
v_mfma_f32_16x16x32_bf16 v[136:139], a[136:139], v[72:75], v[136:139]
v_exp_f32 v247, v247
v_mfma_f32_16x16x32_bf16 v[140:143], a[136:139], v[76:79], v[140:143]
ds_read_b64_tr_b16 a[174:175], v233 offset:6400
v_cvt_pk_bf16_f32 v81, v246, v247
v_mfma_f32_16x16x32_bf16 v[144:147], a[140:143], v[64:67], v[144:147]
v_exp_f32 v244, v244
v_mfma_f32_16x16x32_bf16 v[148:151], a[140:143], v[68:71], v[148:151]
v_exp_f32 v245, v245
v_mfma_f32_16x16x32_bf16 v[152:155], a[140:143], v[72:75], v[152:155]
v_cvt_pk_bf16_f32 v84, v244, v245
v_fma_f32 v246, v38, s27, v241
v_mfma_f32_16x16x32_bf16 v[156:159], a[140:143], v[76:79], v[156:159]
v_fma_f32 v247, v39, s27, v241
v_fma_f32 v244, v40, s27, v242
v_mfma_f32_16x16x32_bf16 v[160:163], a[144:147], v[64:67], v[160:163]
v_exp_f32 v246, v246
v_mfma_f32_16x16x32_bf16 v[164:167], a[144:147], v[68:71], v[164:167]
v_exp_f32 v247, v247
v_mfma_f32_16x16x32_bf16 v[168:171], a[144:147], v[72:75], v[168:171]
v_mfma_f32_16x16x32_bf16 v[172:175], a[144:147], v[76:79], v[172:175]
v_cvt_pk_bf16_f32 v85, v246, v247
v_fma_f32 v245, v41, s27, v242
v_mfma_f32_16x16x32_bf16 v[176:179], a[148:151], v[64:67], v[176:179]
v_exp_f32 v244, v244
v_mfma_f32_16x16x32_bf16 v[180:183], a[148:151], v[68:71], v[180:183]
v_exp_f32 v245, v245
v_mfma_f32_16x16x32_bf16 v[184:187], a[148:151], v[72:75], v[184:187]
v_cvt_pk_bf16_f32 v88, v244, v245
v_mfma_f32_16x16x32_bf16 v[188:191], a[148:151], v[76:79], v[188:191]
v_fma_f32 v246, v42, s27, v242
s_waitcnt lgkmcnt(0)
s_waitcnt vmcnt(18)
s_barrier
v_mfma_f32_16x16x32_bf16 v[192:195], a[152:155], v[64:67], v[192:195]
ds_read_b128 a[128:131], v228 offset:0
v_fma_f32 v247, v43, s27, v242
v_mfma_f32_16x16x32_bf16 v[196:199], a[152:155], v[68:71], v[196:199]
ds_read_b128 a[132:135], v229 offset:0
v_fma_f32 v244, v44, s27, v243
v_mfma_f32_16x16x32_bf16 v[200:203], a[152:155], v[72:75], v[200:203]
ds_read_b128 a[136:139], v228 offset:128
v_fma_f32 v245, v45, s27, v243
v_mfma_f32_16x16x32_bf16 v[204:207], a[152:155], v[76:79], v[204:207]
v_exp_f32 v246, v246
v_mfma_f32_16x16x32_bf16 v[208:211], a[156:159], v[64:67], v[208:211]
ds_read_b128 a[140:143], v229 offset:128
v_mfma_f32_16x16x32_bf16 v[212:215], a[156:159], v[68:71], v[212:215]
ds_read_b128 a[144:147], v228 offset:256
v_mfma_f32_16x16x32_bf16 v[216:219], a[156:159], v[72:75], v[216:219]
v_exp_f32 v247, v247
v_mfma_f32_16x16x32_bf16 v[220:223], a[156:159], v[76:79], v[220:223]
ds_read_b128 a[148:151], v229 offset:256
v_cvt_pk_bf16_f32 v89, v246, v247
v_mfma_f32_16x16x32_bf16 v[224:227], a[160:163], v[64:67], v[224:227]
v_exp_f32 v244, v244
v_mfma_f32_16x16x32_bf16 a[196:199], a[160:163], v[68:71], a[196:199]
s_add_u32 m0, s48, 12288
v_mfma_f32_16x16x32_bf16 a[200:203], a[160:163], v[72:75], a[200:203]
v_exp_f32 v245, v245
v_mfma_f32_16x16x32_bf16 a[204:207], a[160:163], v[76:79], a[204:207]
buffer_load_dwordx4 v234, s[8:11], s24 offen nt lds
v_cvt_pk_bf16_f32 v92, v244, v245
v_mfma_f32_16x16x32_bf16 a[208:211], a[164:167], v[64:67], a[208:211]
v_fma_f32 v246, v46, s27, v243
v_mfma_f32_16x16x32_bf16 a[212:215], a[164:167], v[68:71], a[212:215]
buffer_load_dwordx4 v235, s[8:11], s24 offen offset:1024 nt lds
v_fma_f32 v247, v47, s27, v243
v_mfma_f32_16x16x32_bf16 a[216:219], a[164:167], v[72:75], a[216:219]
v_exp_f32 v246, v246
v_mfma_f32_16x16x32_bf16 a[220:223], a[164:167], v[76:79], a[220:223]
buffer_load_dwordx4 v236, s[8:11], s24 offen offset:2048 nt lds
v_fma_f32 v244, v48, s27, v240
v_mfma_f32_16x16x32_bf16 a[224:227], a[168:171], v[64:67], a[224:227]
v_exp_f32 v247, v247
v_mfma_f32_16x16x32_bf16 a[228:231], a[168:171], v[68:71], a[228:231]
s_add_u32 s24, s24, 12288
v_mfma_f32_16x16x32_bf16 a[232:235], a[168:171], v[72:75], a[232:235]
v_cvt_pk_bf16_f32 v93, v246, v247
v_mfma_f32_16x16x32_bf16 a[236:239], a[168:171], v[76:79], a[236:239]
s_xor_b32 s50, s50, 24576
v_fma_f32 v245, v49, s27, v240
v_mfma_f32_16x16x32_bf16 a[240:243], a[172:175], v[64:67], a[240:243]
v_exp_f32 v244, v244
v_mfma_f32_16x16x32_bf16 a[244:247], a[172:175], v[68:71], a[244:247]
v_exp_f32 v245, v245
v_mfma_f32_16x16x32_bf16 a[248:251], a[172:175], v[72:75], a[248:251]
s_add_u32 s48, s28, s50
v_mfma_f32_16x16x32_bf16 a[252:255], a[172:175], v[76:79], a[252:255]
v_cvt_pk_bf16_f32 v82, v244, v245
v_fma_f32 v246, v50, s27, v240
s_waitcnt lgkmcnt(0)
v_mfma_f32_16x16x32_bf16 v[0:3], a[128:131], a[0:3], 0
ds_read_b128 a[152:155], v228 offset:6144
v_fma_f32 v247, v51, s27, v240
v_mfma_f32_16x16x32_bf16 v[0:3], a[132:135], a[4:7], v[0:3]
s_add_u32 m0, s49, 0
v_mfma_f32_16x16x32_bf16 v[0:3], a[136:139], a[8:11], v[0:3]
ds_read_b128 a[156:159], v229 offset:6144
v_fma_f32 v244, v52, s27, v241
v_mfma_f32_16x16x32_bf16 v[0:3], a[140:143], a[12:15], v[0:3]
buffer_load_dwordx4 v237, s[12:15], s25 offen nt lds
v_fma_f32 v245, v53, s27, v241
v_mfma_f32_16x16x32_bf16 v[0:3], a[144:147], a[16:19], v[0:3]
ds_read_b128 a[160:163], v228 offset:6272
v_mfma_f32_16x16x32_bf16 v[0:3], a[148:151], a[20:23], v[0:3]
v_exp_f32 v246, v246
v_mfma_f32_16x16x32_bf16 v[4:7], a[128:131], a[24:27], 0
ds_read_b128 a[164:167], v229 offset:6272
buffer_load_dwordx4 v238, s[12:15], s25 offen offset:1024 nt lds
v_mfma_f32_16x16x32_bf16 v[4:7], a[132:135], a[28:31], v[4:7]
v_exp_f32 v247, v247
v_mfma_f32_16x16x32_bf16 v[4:7], a[136:139], a[32:35], v[4:7]
ds_read_b128 a[168:171], v228 offset:6400
v_cvt_pk_bf16_f32 v83, v246, v247
v_mfma_f32_16x16x32_bf16 v[4:7], a[140:143], a[36:39], v[4:7]
buffer_load_dwordx4 v239, s[12:15], s25 offen offset:2048 nt lds
v_mfma_f32_16x16x32_bf16 v[4:7], a[144:147], a[40:43], v[4:7]
v_exp_f32 v244, v244
v_mfma_f32_16x16x32_bf16 v[4:7], a[148:151], a[44:47], v[4:7]
ds_read_b128 a[172:175], v229 offset:6400
v_fma_f32 v246, v54, s27, v241
v_mfma_f32_16x16x32_bf16 v[8:11], a[128:131], a[48:51], 0
s_add_u32 s25, s25, 12288
v_fma_f32 v247, v55, s27, v241
v_mfma_f32_16x16x32_bf16 v[8:11], a[132:135], a[52:55], v[8:11]
s_add_u32 m0, s41, 0
v_mfma_f32_16x16x32_bf16 v[8:11], a[136:139], a[56:59], v[8:11]
v_exp_f32 v245, v245
v_mfma_f32_16x16x32_bf16 v[8:11], a[140:143], a[60:63], v[8:11]
v_cvt_pk_bf16_f32 v86, v244, v245
v_fma_f32 v244, v56, s27, v242
v_mfma_f32_16x16x32_bf16 v[8:11], a[144:147], a[64:67], v[8:11]
buffer_load_dwordx4 v250, s[16:19], s40 offen nt lds
v_mfma_f32_16x16x32_bf16 v[8:11], a[148:151], a[68:71], v[8:11]
v_exp_f32 v246, v246
v_mfma_f32_16x16x32_bf16 v[12:15], a[128:131], a[72:75], 0
v_exp_f32 v247, v247
v_mfma_f32_16x16x32_bf16 v[12:15], a[132:135], a[76:79], v[12:15]
s_add_u32 m0, s41, 1024
v_mfma_f32_16x16x32_bf16 v[12:15], a[136:139], a[80:83], v[12:15]
v_cvt_pk_bf16_f32 v87, v246, v247
v_fma_f32 v245, v57, s27, v242
v_mfma_f32_16x16x32_bf16 v[12:15], a[140:143], a[84:87], v[12:15]
v_exp_f32 v244, v244
v_mfma_f32_16x16x32_bf16 v[12:15], a[144:147], a[88:91], v[12:15]
v_exp_f32 v245, v245
v_mfma_f32_16x16x32_bf16 v[12:15], a[148:151], a[92:95], v[12:15]
buffer_load_dwordx4 v251, s[16:19], s40 offen nt lds
s_waitcnt lgkmcnt(0)
v_mfma_f32_16x16x32_bf16 v[16:19], a[152:155], a[0:3], 0
ds_read_b64_tr_b16 a[128:129], v230 offset:12288
v_cvt_pk_bf16_f32 v90, v244, v245
v_mfma_f32_16x16x32_bf16 v[16:19], a[156:159], a[4:7], v[16:19]
ds_read_b64_tr_b16 a[130:131], v230 offset:18432
v_mfma_f32_16x16x32_bf16 v[16:19], a[160:163], a[8:11], v[16:19]
ds_read_b64_tr_b16 a[132:133], v231 offset:12288
v_fma_f32 v246, v58, s27, v242
v_mfma_f32_16x16x32_bf16 v[16:19], a[164:167], a[12:15], v[16:19]
v_fma_f32 v247, v59, s27, v242
v_fma_f32 v244, v60, s27, v243
v_mfma_f32_16x16x32_bf16 v[16:19], a[168:171], a[16:19], v[16:19]
ds_read_b64_tr_b16 a[134:135], v231 offset:18432
v_fma_f32 v245, v61, s27, v243
v_mfma_f32_16x16x32_bf16 v[16:19], a[172:175], a[20:23], v[16:19]
ds_read_b64_tr_b16 a[136:137], v232 offset:12288
v_mfma_f32_16x16x32_bf16 v[20:23], a[152:155], a[24:27], 0
ds_read_b64_tr_b16 a[138:139], v232 offset:18432
v_mfma_f32_16x16x32_bf16 v[20:23], a[156:159], a[28:31], v[20:23]
v_exp_f32 v246, v246
v_mfma_f32_16x16x32_bf16 v[20:23], a[160:163], a[32:35], v[20:23]
ds_read_b64_tr_b16 a[140:141], v233 offset:12288
v_mfma_f32_16x16x32_bf16 v[20:23], a[164:167], a[36:39], v[20:23]
ds_read_b64_tr_b16 a[142:143], v233 offset:18432
v_mfma_f32_16x16x32_bf16 v[20:23], a[168:171], a[40:43], v[20:23]
ds_read_b64_tr_b16 a[144:145], v230 offset:12416
v_mfma_f32_16x16x32_bf16 v[20:23], a[172:175], a[44:47], v[20:23]
v_exp_f32 v247, v247
v_mfma_f32_16x16x32_bf16 v[24:27], a[152:155], a[48:51], 0
ds_read_b64_tr_b16 a[146:147], v230 offset:18560
v_cvt_pk_bf16_f32 v91, v246, v247
v_mfma_f32_16x16x32_bf16 v[24:27], a[156:159], a[52:55], v[24:27]
ds_read_b64_tr_b16 a[148:149], v231 offset:12416
v_fma_f32 v246, v62, s27, v243
v_mfma_f32_16x16x32_bf16 v[24:27], a[160:163], a[56:59], v[24:27]
v_exp_f32 v244, v244
v_mfma_f32_16x16x32_bf16 v[24:27], a[164:167], a[60:63], v[24:27]
ds_read_b64_tr_b16 a[150:151], v231 offset:18560
v_fma_f32 v247, v63, s27, v243
v_mfma_f32_16x16x32_bf16 v[24:27], a[168:171], a[64:67], v[24:27]
v_exp_f32 v245, v245
v_mfma_f32_16x16x32_bf16 v[24:27], a[172:175], a[68:71], v[24:27]
v_cvt_pk_bf16_f32 v94, v244, v245
v_mfma_f32_16x16x32_bf16 v[28:31], a[152:155], a[72:75], 0
v_exp_f32 v246, v246
v_mfma_f32_16x16x32_bf16 v[28:31], a[156:159], a[76:79], v[28:31]
v_exp_f32 v247, v247
v_mfma_f32_16x16x32_bf16 v[28:31], a[160:163], a[80:83], v[28:31]
v_cvt_pk_bf16_f32 v95, v246, v247
v_mfma_f32_16x16x32_bf16 v[28:31], a[164:167], a[84:87], v[28:31]
v_mfma_f32_16x16x32_bf16 v[28:31], a[168:171], a[88:91], v[28:31]
v_mfma_f32_16x16x32_bf16 v[28:31], a[172:175], a[92:95], v[28:31]
; ==== period L1 parity 0
s_waitcnt lgkmcnt(0)
v_mfma_f32_16x16x32_bf16 a[176:179], a[192:195], v[80:83], a[176:179]
ds_read_b64_tr_b16 a[152:153], v232 offset:12416
v_fma_f32 v244, v0, s27, v240
v_mfma_f32_16x16x32_bf16 a[180:183], a[192:195], v[84:87], a[180:183]
ds_read_b64_tr_b16 a[154:155], v232 offset:18560
v_fma_f32 v245, v1, s27, v240
v_mfma_f32_16x16x32_bf16 a[184:187], a[192:195], v[88:91], a[184:187]
ds_read_b64_tr_b16 a[156:157], v233 offset:12416
v_fma_f32 v246, v2, s27, v240
v_mfma_f32_16x16x32_bf16 a[188:191], a[192:195], v[92:95], a[188:191]
v_exp_f32 v244, v244
v_mfma_f32_16x16x32_bf16 v[96:99], a[128:131], v[80:83], v[96:99]
ds_read_b64_tr_b16 a[158:159], v233 offset:18560
v_fma_f32 v247, v3, s27, v240
v_mfma_f32_16x16x32_bf16 v[100:103], a[128:131], v[84:87], v[100:103]
ds_read_b64_tr_b16 a[160:161], v230 offset:12544
v_mfma_f32_16x16x32_bf16 v[104:107], a[128:131], v[88:91], v[104:107]
ds_read_b64_tr_b16 a[162:163], v230 offset:18688
v_mfma_f32_16x16x32_bf16 v[108:111], a[128:131], v[92:95], v[108:111]
v_exp_f32 v245, v245
v_mfma_f32_16x16x32_bf16 v[112:115], a[132:135], v[80:83], v[112:115]
ds_read_b64_tr_b16 a[164:165], v231 offset:12544
v_cvt_pk_bf16_f32 v64, v244, v245
v_mfma_f32_16x16x32_bf16 v[116:119], a[132:135], v[84:87], v[116:119]
ds_read_b64_tr_b16 a[166:167], v231 offset:18688
v_fma_f32 v244, v4, s27, v241
v_mfma_f32_16x16x32_bf16 v[120:123], a[132:135], v[88:91], v[120:123]
ds_read_b64_tr_b16 a[168:169], v232 offset:12544
v_fma_f32 v245, v5, s27, v241
v_mfma_f32_16x16x32_bf16 v[124:127], a[132:135], v[92:95], v[124:127]
v_exp_f32 v246, v246
v_mfma_f32_16x16x32_bf16 v[128:131], a[136:139], v[80:83], v[128:131]
ds_read_b64_tr_b16 a[170:171], v232 offset:18688
v_mfma_f32_16x16x32_bf16 v[132:135], a[136:139], v[84:87], v[132:135]
ds_read_b64_tr_b16 a[172:173], v233 offset:12544
v_mfma_f32_16x16x32_bf16 v[136:139], a[136:139], v[88:91], v[136:139]
v_exp_f32 v247, v247
v_mfma_f32_16x16x32_bf16 v[140:143], a[136:139], v[92:95], v[140:143]
ds_read_b64_tr_b16 a[174:175], v233 offset:18688
v_cvt_pk_bf16_f32 v65, v246, v247
v_mfma_f32_16x16x32_bf16 v[144:147], a[140:143], v[80:83], v[144:147]
v_exp_f32 v244, v244
v_mfma_f32_16x16x32_bf16 v[148:151], a[140:143], v[84:87], v[148:151]
v_exp_f32 v245, v245
v_mfma_f32_16x16x32_bf16 v[152:155], a[140:143], v[88:91], v[152:155]
v_cvt_pk_bf16_f32 v68, v244, v245
v_fma_f32 v246, v6, s27, v241
v_mfma_f32_16x16x32_bf16 v[156:159], a[140:143], v[92:95], v[156:159]
v_xor_b32 v230, 24576, v230
v_fma_f32 v247, v7, s27, v241
v_mfma_f32_16x16x32_bf16 v[160:163], a[144:147], v[80:83], v[160:163]
v_exp_f32 v246, v246
v_mfma_f32_16x16x32_bf16 v[164:167], a[144:147], v[84:87], v[164:167]
v_xor_b32 v231, 24576, v231
v_fma_f32 v244, v8, s27, v242
v_mfma_f32_16x16x32_bf16 v[168:171], a[144:147], v[88:91], v[168:171]
v_exp_f32 v247, v247
v_mfma_f32_16x16x32_bf16 v[172:175], a[144:147], v[92:95], v[172:175]
v_xor_b32 v232, 24576, v232
v_cvt_pk_bf16_f32 v69, v246, v247
v_mfma_f32_16x16x32_bf16 v[176:179], a[148:151], v[80:83], v[176:179]
v_fma_f32 v245, v9, s27, v242
v_fma_f32 v246, v10, s27, v242
v_mfma_f32_16x16x32_bf16 v[180:183], a[148:151], v[84:87], v[180:183]
v_xor_b32 v233, 24576, v233
v_fma_f32 v247, v11, s27, v242
v_mfma_f32_16x16x32_bf16 v[184:187], a[148:151], v[88:91], v[184:187]
v_exp_f32 v244, v244
v_mfma_f32_16x16x32_bf16 v[188:191], a[148:151], v[92:95], v[188:191]
v_exp_f32 v245, v245
s_waitcnt lgkmcnt(0)
s_waitcnt vmcnt(18)
s_barrier
v_mfma_f32_16x16x32_bf16 v[192:195], a[152:155], v[80:83], v[192:195]
ds_read_b128 a[128:131], v228 offset:12288
v_mfma_f32_16x16x32_bf16 v[196:199], a[152:155], v[84:87], v[196:199]
ds_read_b128 a[132:135], v229 offset:12288
v_cvt_pk_bf16_f32 v72, v244, v245
v_mfma_f32_16x16x32_bf16 v[200:203], a[152:155], v[88:91], v[200:203]
ds_read_b128 a[136:139], v228 offset:12416
v_fma_f32 v244, v12, s27, v243
v_mfma_f32_16x16x32_bf16 v[204:207], a[152:155], v[92:95], v[204:207]
v_exp_f32 v246, v246
v_mfma_f32_16x16x32_bf16 v[208:211], a[156:159], v[80:83], v[208:211]
ds_read_b128 a[140:143], v229 offset:12416
v_fma_f32 v245, v13, s27, v243
v_mfma_f32_16x16x32_bf16 v[212:215], a[156:159], v[84:87], v[212:215]
ds_read_b128 a[144:147], v228 offset:12544
v_mfma_f32_16x16x32_bf16 v[216:219], a[156:159], v[88:91], v[216:219]
v_exp_f32 v247, v247
v_mfma_f32_16x16x32_bf16 v[220:223], a[156:159], v[92:95], v[220:223]
ds_read_b128 a[148:151], v229 offset:12544
v_cvt_pk_bf16_f32 v73, v246, v247
v_mfma_f32_16x16x32_bf16 v[224:227], a[160:163], v[80:83], v[224:227]
v_exp_f32 v244, v244
v_mfma_f32_16x16x32_bf16 a[196:199], a[160:163], v[84:87], a[196:199]
s_add_u32 m0, s48, 0
v_mfma_f32_16x16x32_bf16 a[200:203], a[160:163], v[88:91], a[200:203]
v_exp_f32 v245, v245
v_mfma_f32_16x16x32_bf16 a[204:207], a[160:163], v[92:95], a[204:207]
v_cvt_pk_bf16_f32 v76, v244, v245
v_fma_f32 v246, v14, s27, v243
v_mfma_f32_16x16x32_bf16 a[208:211], a[164:167], v[80:83], a[208:211]
buffer_load_dwordx4 v234, s[8:11], s24 offen nt lds
v_fma_f32 v247, v15, s27, v243
v_mfma_f32_16x16x32_bf16 a[212:215], a[164:167], v[84:87], a[212:215]
v_exp_f32 v246, v246
v_mfma_f32_16x16x32_bf16 a[216:219], a[164:167], v[88:91], a[216:219]
v_exp_f32 v247, v247
v_mfma_f32_16x16x32_bf16 a[220:223], a[164:167], v[92:95], a[220:223]
buffer_load_dwordx4 v235, s[8:11], s24 offen offset:1024 nt lds
v_mfma_f32_16x16x32_bf16 a[224:227], a[168:171], v[80:83], a[224:227]
v_cvt_pk_bf16_f32 v77, v246, v247
v_fma_f32 v244, v16, s27, v240
v_mfma_f32_16x16x32_bf16 a[228:231], a[168:171], v[84:87], a[228:231]
v_fma_f32 v245, v17, s27, v240
v_mfma_f32_16x16x32_bf16 a[232:235], a[168:171], v[88:91], a[232:235]
buffer_load_dwordx4 v236, s[8:11], s24 offen offset:2048 nt lds
v_fma_f32 v246, v18, s27, v240
v_mfma_f32_16x16x32_bf16 a[236:239], a[168:171], v[92:95], a[236:239]
v_exp_f32 v244, v244
v_mfma_f32_16x16x32_bf16 a[240:243], a[172:175], v[80:83], a[240:243]
v_exp_f32 v245, v245
v_mfma_f32_16x16x32_bf16 a[244:247], a[172:175], v[84:87], a[244:247]
v_cvt_pk_bf16_f32 v66, v244, v245
v_mfma_f32_16x16x32_bf16 a[248:251], a[172:175], v[88:91], a[248:251]
s_add_u32 s24, s24, 12288
v_fma_f32 v247, v19, s27, v240
v_mfma_f32_16x16x32_bf16 a[252:255], a[172:175], v[92:95], a[252:255]
v_exp_f32 v246, v246
s_waitcnt lgkmcnt(0)
v_mfma_f32_16x16x32_bf16 v[32:35], a[128:131], a[0:3], 0
ds_read_b128 a[152:155], v228 offset:18432
v_fma_f32 v244, v20, s27, v241
v_mfma_f32_16x16x32_bf16 v[32:35], a[132:135], a[4:7], v[32:35]
s_add_u32 m0, s49, 12288
v_fma_f32 v245, v21, s27, v241
v_mfma_f32_16x16x32_bf16 v[32:35], a[136:139], a[8:11], v[32:35]
ds_read_b128 a[156:159], v229 offset:18432
buffer_load_dwordx4 v237, s[12:15], s25 offen nt lds
v_mfma_f32_16x16x32_bf16 v[32:35], a[140:143], a[12:15], v[32:35]
v_exp_f32 v247, v247
v_mfma_f32_16x16x32_bf16 v[32:35], a[144:147], a[16:19], v[32:35]
ds_read_b128 a[160:163], v228 offset:18560
buffer_load_dwordx4 v238, s[12:15], s25 offen offset:1024 nt lds
v_mfma_f32_16x16x32_bf16 v[32:35], a[148:151], a[20:23], v[32:35]
v_cvt_pk_bf16_f32 v67, v246, v247
v_mfma_f32_16x16x32_bf16 v[36:39], a[128:131], a[24:27], 0
ds_read_b128 a[164:167], v229 offset:18560
buffer_load_dwordx4 v239, s[12:15], s25 offen offset:2048 nt lds
v_mfma_f32_16x16x32_bf16 v[36:39], a[132:135], a[28:31], v[36:39]
v_exp_f32 v244, v244
v_mfma_f32_16x16x32_bf16 v[36:39], a[136:139], a[32:35], v[36:39]
ds_read_b128 a[168:171], v228 offset:18688
s_add_u32 s25, s25, 12288
v_mfma_f32_16x16x32_bf16 v[36:39], a[140:143], a[36:39], v[36:39]
v_exp_f32 v245, v245
v_mfma_f32_16x16x32_bf16 v[36:39], a[144:147], a[40:43], v[36:39]
s_xor_b32 s51, s51, 24576
v_mfma_f32_16x16x32_bf16 v[36:39], a[148:151], a[44:47], v[36:39]
ds_read_b128 a[172:175], v229 offset:18688
v_cvt_pk_bf16_f32 v70, v244, v245
v_mfma_f32_16x16x32_bf16 v[40:43], a[128:131], a[48:51], 0
s_add_u32 s49, s29, s51
v_fma_f32 v246, v22, s27, v241
v_mfma_f32_16x16x32_bf16 v[40:43], a[132:135], a[52:55], v[40:43]
v_xor_b32 v228, 24576, v228
s_add_u32 m0, s41, 1920
v_mfma_f32_16x16x32_bf16 v[40:43], a[136:139], a[56:59], v[40:43]
buffer_load_dwordx4 v250, s[16:19], s40 offen offset:128 nt lds
v_fma_f32 v247, v23, s27, v241
v_mfma_f32_16x16x32_bf16 v[40:43], a[140:143], a[60:63], v[40:43]
v_xor_b32 v229, 24576, v229
v_fma_f32 v244, v24, s27, v242
v_mfma_f32_16x16x32_bf16 v[40:43], a[144:147], a[64:67], v[40:43]
s_add_u32 m0, s41, 2944
v_mfma_f32_16x16x32_bf16 v[40:43], a[148:151], a[68:71], v[40:43]
v_exp_f32 v246, v246
v_mfma_f32_16x16x32_bf16 v[44:47], a[128:131], a[72:75], 0
buffer_load_dwordx4 v251, s[16:19], s40 offen offset:128 nt lds
v_fma_f32 v245, v25, s27, v242
v_mfma_f32_16x16x32_bf16 v[44:47], a[132:135], a[76:79], v[44:47]
s_add_u32 s40, s40, 6144
v_mfma_f32_16x16x32_bf16 v[44:47], a[136:139], a[80:83], v[44:47]
v_exp_f32 v247, v247
v_mfma_f32_16x16x32_bf16 v[44:47], a[140:143], a[84:87], v[44:47]
s_add_u32 s41, s41, 4096
v_cvt_pk_bf16_f32 v71, v246, v247
v_mfma_f32_16x16x32_bf16 v[44:47], a[144:147], a[88:91], v[44:47]
v_exp_f32 v244, v244
v_mfma_f32_16x16x32_bf16 v[44:47], a[148:151], a[92:95], v[44:47]
s_cmp_eq_u32 s52, 4
s_cselect_b32 s41, 163840, s41
s_cselect_b32 s40, s55, s40
s_waitcnt lgkmcnt(0)
v_mfma_f32_16x16x32_bf16 v[48:51], a[152:155], a[0:3], 0
ds_read_b64_tr_b16 a[128:129], v230 offset:0
v_fma_f32 v246, v26, s27, v242
v_mfma_f32_16x16x32_bf16 v[48:51], a[156:159], a[4:7], v[48:51]
ds_read_b64_tr_b16 a[130:131], v230 offset:6144
v_fma_f32 v247, v27, s27, v242
v_mfma_f32_16x16x32_bf16 v[48:51], a[160:163], a[8:11], v[48:51]
ds_read_b64_tr_b16 a[132:133], v231 offset:0
v_mfma_f32_16x16x32_bf16 v[48:51], a[164:167], a[12:15], v[48:51]
v_exp_f32 v245, v245
v_mfma_f32_16x16x32_bf16 v[48:51], a[168:171], a[16:19], v[48:51]
ds_read_b64_tr_b16 a[134:135], v231 offset:6144
v_cvt_pk_bf16_f32 v74, v244, v245
v_mfma_f32_16x16x32_bf16 v[48:51], a[172:175], a[20:23], v[48:51]
ds_read_b64_tr_b16 a[136:137], v232 offset:0
v_fma_f32 v244, v28, s27, v243
v_mfma_f32_16x16x32_bf16 v[52:55], a[152:155], a[24:27], 0
ds_read_b64_tr_b16 a[138:139], v232 offset:6144
v_fma_f32 v245, v29, s27, v243
v_mfma_f32_16x16x32_bf16 v[52:55], a[156:159], a[28:31], v[52:55]
v_exp_f32 v246, v246
v_mfma_f32_16x16x32_bf16 v[52:55], a[160:163], a[32:35], v[52:55]
ds_read_b64_tr_b16 a[140:141], v233 offset:0
v_mfma_f32_16x16x32_bf16 v[52:55], a[164:167], a[36:39], v[52:55]
ds_read_b64_tr_b16 a[142:143], v233 offset:6144
v_mfma_f32_16x16x32_bf16 v[52:55], a[168:171], a[40:43], v[52:55]
ds_read_b64_tr_b16 a[144:145], v230 offset:128
v_mfma_f32_16x16x32_bf16 v[52:55], a[172:175], a[44:47], v[52:55]
v_exp_f32 v247, v247
v_mfma_f32_16x16x32_bf16 v[56:59], a[152:155], a[48:51], 0
ds_read_b64_tr_b16 a[146:147], v230 offset:6272
v_cvt_pk_bf16_f32 v75, v246, v247
v_mfma_f32_16x16x32_bf16 v[56:59], a[156:159], a[52:55], v[56:59]
ds_read_b64_tr_b16 a[148:149], v231 offset:128
v_fma_f32 v246, v30, s27, v243
v_mfma_f32_16x16x32_bf16 v[56:59], a[160:163], a[56:59], v[56:59]
v_exp_f32 v244, v244
v_mfma_f32_16x16x32_bf16 v[56:59], a[164:167], a[60:63], v[56:59]
ds_read_b64_tr_b16 a[150:151], v231 offset:6272
v_fma_f32 v247, v31, s27, v243
v_mfma_f32_16x16x32_bf16 v[56:59], a[168:171], a[64:67], v[56:59]
v_exp_f32 v245, v245
v_mfma_f32_16x16x32_bf16 v[56:59], a[172:175], a[68:71], v[56:59]
v_cvt_pk_bf16_f32 v78, v244, v245
v_mfma_f32_16x16x32_bf16 v[60:63], a[152:155], a[72:75], 0
v_exp_f32 v246, v246
v_mfma_f32_16x16x32_bf16 v[60:63], a[156:159], a[76:79], v[60:63]
v_exp_f32 v247, v247
v_mfma_f32_16x16x32_bf16 v[60:63], a[160:163], a[80:83], v[60:63]
v_cvt_pk_bf16_f32 v79, v246, v247
v_mfma_f32_16x16x32_bf16 v[60:63], a[164:167], a[84:87], v[60:63]
v_mfma_f32_16x16x32_bf16 v[60:63], a[168:171], a[88:91], v[60:63]
v_mfma_f32_16x16x32_bf16 v[60:63], a[172:175], a[92:95], v[60:63]
s_sub_u32 s52, s52, 1
s_cmp_lg_u32 s52, 0
s_cbranch_scc1 LOOP1_%=
; ==== period 15
s_waitcnt lgkmcnt(0)
v_mfma_f32_16x16x32_bf16 a[176:179], a[192:195], v[64:67], a[176:179]
ds_read_b64_tr_b16 a[152:153], v232 offset:128
v_fma_f32 v244, v32, s27, v240
v_fma_f32 v245, v33, s27, v240
v_mfma_f32_16x16x32_bf16 a[180:183], a[192:195], v[68:71], a[180:183]
ds_read_b64_tr_b16 a[154:155], v232 offset:6272
v_exp_f32 v244, v244
v_mfma_f32_16x16x32_bf16 a[184:187], a[192:195], v[72:75], a[184:187]
ds_read_b64_tr_b16 a[156:157], v233 offset:128
v_mfma_f32_16x16x32_bf16 a[188:191], a[192:195], v[76:79], a[188:191]
v_exp_f32 v245, v245
v_mfma_f32_16x16x32_bf16 v[96:99], a[128:131], v[64:67], v[96:99]
ds_read_b64_tr_b16 a[158:159], v233 offset:6272
v_cvt_pk_bf16_f32 v80, v244, v245
v_mfma_f32_16x16x32_bf16 v[100:103], a[128:131], v[68:71], v[100:103]
ds_read_b64_tr_b16 a[160:161], v230 offset:256
v_fma_f32 v246, v34, s27, v240
v_mfma_f32_16x16x32_bf16 v[104:107], a[128:131], v[72:75], v[104:107]
ds_read_b64_tr_b16 a[162:163], v230 offset:6400
v_fma_f32 v247, v35, s27, v240
v_mfma_f32_16x16x32_bf16 v[108:111], a[128:131], v[76:79], v[108:111]
v_exp_f32 v246, v246
v_mfma_f32_16x16x32_bf16 v[112:115], a[132:135], v[64:67], v[112:115]
ds_read_b64_tr_b16 a[164:165], v231 offset:256
v_mfma_f32_16x16x32_bf16 v[116:119], a[132:135], v[68:71], v[116:119]
ds_read_b64_tr_b16 a[166:167], v231 offset:6400
v_exp_f32 v247, v247
v_mfma_f32_16x16x32_bf16 v[120:123], a[132:135], v[72:75], v[120:123]
ds_read_b64_tr_b16 a[168:169], v232 offset:256
v_mfma_f32_16x16x32_bf16 v[124:127], a[132:135], v[76:79], v[124:127]
v_cvt_pk_bf16_f32 v81, v246, v247
v_fma_f32 v244, v36, s27, v241
v_mfma_f32_16x16x32_bf16 v[128:131], a[136:139], v[64:67], v[128:131]
ds_read_b64_tr_b16 a[170:171], v232 offset:6400
v_fma_f32 v245, v37, s27, v241
v_mfma_f32_16x16x32_bf16 v[132:135], a[136:139], v[68:71], v[132:135]
ds_read_b64_tr_b16 a[172:173], v233 offset:256
ds_read_b128 a[0:3], v252 offset:0
v_mfma_f32_16x16x32_bf16 v[136:139], a[136:139], v[72:75], v[136:139]
ds_read_b128 a[4:7], v253 offset:0
v_exp_f32 v244, v244
v_mfma_f32_16x16x32_bf16 v[140:143], a[136:139], v[76:79], v[140:143]
ds_read_b64_tr_b16 a[174:175], v233 offset:6400
v_mfma_f32_16x16x32_bf16 v[144:147], a[140:143], v[64:67], v[144:147]
ds_read_b128 a[8:11], v252 offset:2048
v_exp_f32 v245, v245
v_mfma_f32_16x16x32_bf16 v[148:151], a[140:143], v[68:71], v[148:151]
v_cvt_pk_bf16_f32 v84, v244, v245
v_mfma_f32_16x16x32_bf16 v[152:155], a[140:143], v[72:75], v[152:155]
ds_read_b128 a[12:15], v253 offset:2048
v_fma_f32 v246, v38, s27, v241
v_mfma_f32_16x16x32_bf16 v[156:159], a[140:143], v[76:79], v[156:159]
v_fma_f32 v247, v39, s27, v241
v_mfma_f32_16x16x32_bf16 v[160:163], a[144:147], v[64:67], v[160:163]
ds_read_b128 a[24:27], v252 offset:4096
v_exp_f32 v246, v246
v_mfma_f32_16x16x32_bf16 v[164:167], a[144:147], v[68:71], v[164:167]
v_exp_f32 v247, v247
v_mfma_f32_16x16x32_bf16 v[168:171], a[144:147], v[72:75], v[168:171]
ds_read_b128 a[28:31], v253 offset:4096
v_mfma_f32_16x16x32_bf16 v[172:175], a[144:147], v[76:79], v[172:175]
v_cvt_pk_bf16_f32 v85, v246, v247
v_mfma_f32_16x16x32_bf16 v[176:179], a[148:151], v[64:67], v[176:179]
ds_read_b128 a[32:35], v252 offset:6144
v_fma_f32 v244, v40, s27, v242
v_mfma_f32_16x16x32_bf16 v[180:183], a[148:151], v[68:71], v[180:183]
v_fma_f32 v245, v41, s27, v242
v_exp_f32 v244, v244
v_mfma_f32_16x16x32_bf16 v[184:187], a[148:151], v[72:75], v[184:187]
ds_read_b128 a[36:39], v253 offset:6144
v_mfma_f32_16x16x32_bf16 v[188:191], a[148:151], v[76:79], v[188:191]
v_exp_f32 v245, v245
s_waitcnt lgkmcnt(6)
s_waitcnt vmcnt(18)
s_barrier
v_mfma_f32_16x16x32_bf16 v[192:195], a[152:155], v[64:67], v[192:195]
ds_read_b128 a[128:131], v228 offset:0
v_mfma_f32_16x16x32_bf16 v[196:199], a[152:155], v[68:71], v[196:199]
ds_read_b128 a[132:135], v229 offset:0
v_cvt_pk_bf16_f32 v88, v244, v245
v_mfma_f32_16x16x32_bf16 v[200:203], a[152:155], v[72:75], v[200:203]
ds_read_b128 a[136:139], v228 offset:128
v_fma_f32 v246, v42, s27, v242
v_mfma_f32_16x16x32_bf16 v[204:207], a[152:155], v[76:79], v[204:207]
v_fma_f32 v247, v43, s27, v242
v_exp_f32 v246, v246
v_mfma_f32_16x16x32_bf16 v[208:211], a[156:159], v[64:67], v[208:211]
ds_read_b128 a[140:143], v229 offset:128
v_mfma_f32_16x16x32_bf16 v[212:215], a[156:159], v[68:71], v[212:215]
ds_read_b128 a[144:147], v228 offset:256
v_mfma_f32_16x16x32_bf16 v[216:219], a[156:159], v[72:75], v[216:219]
v_exp_f32 v247, v247
v_mfma_f32_16x16x32_bf16 v[220:223], a[156:159], v[76:79], v[220:223]
ds_read_b128 a[148:151], v229 offset:256
v_cvt_pk_bf16_f32 v89, v246, v247
v_mfma_f32_16x16x32_bf16 v[224:227], a[160:163], v[64:67], v[224:227]
v_fma_f32 v244, v44, s27, v243
v_mfma_f32_16x16x32_bf16 a[196:199], a[160:163], v[68:71], a[196:199]
s_add_u32 m0, s48, 12288
ds_read_b128 a[48:51], v252 offset:8192
v_fma_f32 v245, v45, s27, v243
v_mfma_f32_16x16x32_bf16 a[200:203], a[160:163], v[72:75], a[200:203]
ds_read_b128 a[52:55], v253 offset:8192
v_exp_f32 v244, v244
v_mfma_f32_16x16x32_bf16 a[204:207], a[160:163], v[76:79], a[204:207]
buffer_load_dwordx4 v234, s[8:11], s24 offen nt lds
v_mfma_f32_16x16x32_bf16 a[208:211], a[164:167], v[64:67], a[208:211]
ds_read_b128 a[56:59], v252 offset:10240
v_exp_f32 v245, v245
v_mfma_f32_16x16x32_bf16 a[212:215], a[164:167], v[68:71], a[212:215]
buffer_load_dwordx4 v235, s[8:11], s24 offen offset:1024 nt lds
ds_read_b128 a[60:63], v253 offset:10240
v_mfma_f32_16x16x32_bf16 a[216:219], a[164:167], v[72:75], a[216:219]
v_cvt_pk_bf16_f32 v92, v244, v245
v_mfma_f32_16x16x32_bf16 a[220:223], a[164:167], v[76:79], a[220:223]
buffer_load_dwordx4 v236, s[8:11], s24 offen offset:2048 nt lds
ds_read_b128 a[72:75], v252 offset:12288
v_mfma_f32_16x16x32_bf16 a[224:227], a[168:171], v[64:67], a[224:227]
ds_read_b128 a[76:79], v253 offset:12288
v_fma_f32 v246, v46, s27, v243
v_mfma_f32_16x16x32_bf16 a[228:231], a[168:171], v[68:71], a[228:231]
s_add_u32 s24, s24, 12288
v_fma_f32 v247, v47, s27, v243
v_mfma_f32_16x16x32_bf16 a[232:235], a[168:171], v[72:75], a[232:235]
ds_read_b128 a[80:83], v252 offset:14336
v_exp_f32 v246, v246
v_mfma_f32_16x16x32_bf16 a[236:239], a[168:171], v[76:79], a[236:239]
s_xor_b32 s50, s50, 24576
v_mfma_f32_16x16x32_bf16 a[240:243], a[172:175], v[64:67], a[240:243]
ds_read_b128 a[84:87], v253 offset:14336
v_exp_f32 v247, v247
v_mfma_f32_16x16x32_bf16 a[244:247], a[172:175], v[68:71], a[244:247]
v_cvt_pk_bf16_f32 v93, v246, v247
v_mfma_f32_16x16x32_bf16 a[248:251], a[172:175], v[72:75], a[248:251]
s_add_u32 s48, s28, s50
v_fma_f32 v244, v48, s27, v240
v_mfma_f32_16x16x32_bf16 a[252:255], a[172:175], v[76:79], a[252:255]
v_fma_f32 v245, v49, s27, v240
s_waitcnt lgkmcnt(0)
v_mfma_f32_16x16x32_bf16 v[0:3], a[128:131], a[0:3], 0
ds_read_b128 a[152:155], v228 offset:6144
v_exp_f32 v244, v244
v_mfma_f32_16x16x32_bf16 v[0:3], a[132:135], a[4:7], v[0:3]
s_add_u32 m0, s49, 0
v_mfma_f32_16x16x32_bf16 v[0:3], a[136:139], a[8:11], v[0:3]
ds_read_b128 a[156:159], v229 offset:6144
v_exp_f32 v245, v245
v_mfma_f32_16x16x32_bf16 v[0:3], a[140:143], a[12:15], v[0:3]
buffer_load_dwordx4 v237, s[12:15], s25 offen nt lds
v_mfma_f32_16x16x32_bf16 v[0:3], a[144:147], a[96:99], v[0:3]
ds_read_b128 a[160:163], v228 offset:6272
v_cvt_pk_bf16_f32 v82, v244, v245
v_mfma_f32_16x16x32_bf16 v[0:3], a[148:151], a[100:103], v[0:3]
v_fma_f32 v246, v50, s27, v240
v_fma_f32 v247, v51, s27, v240
v_mfma_f32_16x16x32_bf16 v[4:7], a[128:131], a[24:27], 0
ds_read_b128 a[164:167], v229 offset:6272
buffer_load_dwordx4 v238, s[12:15], s25 offen offset:1024 nt lds
v_mfma_f32_16x16x32_bf16 v[4:7], a[132:135], a[28:31], v[4:7]
v_exp_f32 v246, v246
v_mfma_f32_16x16x32_bf16 v[4:7], a[136:139], a[32:35], v[4:7]
ds_read_b128 a[168:171], v228 offset:6400
v_exp_f32 v247, v247
v_mfma_f32_16x16x32_bf16 v[4:7], a[140:143], a[36:39], v[4:7]
buffer_load_dwordx4 v239, s[12:15], s25 offen offset:2048 nt lds
v_mfma_f32_16x16x32_bf16 v[4:7], a[144:147], a[104:107], v[4:7]
v_cvt_pk_bf16_f32 v83, v246, v247
v_mfma_f32_16x16x32_bf16 v[4:7], a[148:151], a[108:111], v[4:7]
ds_read_b128 a[172:175], v229 offset:6400
v_fma_f32 v244, v52, s27, v241
v_mfma_f32_16x16x32_bf16 v[8:11], a[128:131], a[48:51], 0
s_add_u32 s25, s25, 12288
v_fma_f32 v245, v53, s27, v241
v_mfma_f32_16x16x32_bf16 v[8:11], a[132:135], a[52:55], v[8:11]
v_exp_f32 v244, v244
v_mfma_f32_16x16x32_bf16 v[8:11], a[136:139], a[56:59], v[8:11]
v_exp_f32 v245, v245
v_mfma_f32_16x16x32_bf16 v[8:11], a[140:143], a[60:63], v[8:11]
v_mfma_f32_16x16x32_bf16 v[8:11], a[144:147], a[112:115], v[8:11]
v_cvt_pk_bf16_f32 v86, v244, v245
v_fma_f32 v246, v54, s27, v241
v_mfma_f32_16x16x32_bf16 v[8:11], a[148:151], a[116:119], v[8:11]
v_fma_f32 v247, v55, s27, v241
v_mfma_f32_16x16x32_bf16 v[12:15], a[128:131], a[72:75], 0
v_exp_f32 v246, v246
v_mfma_f32_16x16x32_bf16 v[12:15], a[132:135], a[76:79], v[12:15]
v_exp_f32 v247, v247
v_mfma_f32_16x16x32_bf16 v[12:15], a[136:139], a[80:83], v[12:15]
v_cvt_pk_bf16_f32 v87, v246, v247
v_mfma_f32_16x16x32_bf16 v[12:15], a[140:143], a[84:87], v[12:15]
v_fma_f32 v244, v56, s27, v242
v_mfma_f32_16x16x32_bf16 v[12:15], a[144:147], a[120:123], v[12:15]
v_fma_f32 v245, v57, s27, v242
v_exp_f32 v244, v244
v_mfma_f32_16x16x32_bf16 v[12:15], a[148:151], a[124:127], v[12:15]
s_waitcnt lgkmcnt(0)
v_mfma_f32_16x16x32_bf16 v[16:19], a[152:155], a[0:3], 0
ds_read_b64_tr_b16 a[128:129], v230 offset:12288
v_exp_f32 v245, v245
v_mfma_f32_16x16x32_bf16 v[16:19], a[156:159], a[4:7], v[16:19]
ds_read_b64_tr_b16 a[130:131], v230 offset:18432
v_mfma_f32_16x16x32_bf16 v[16:19], a[160:163], a[8:11], v[16:19]
ds_read_b64_tr_b16 a[132:133], v231 offset:12288
v_cvt_pk_bf16_f32 v90, v244, v245
v_mfma_f32_16x16x32_bf16 v[16:19], a[164:167], a[12:15], v[16:19]
v_fma_f32 v246, v58, s27, v242
v_fma_f32 v247, v59, s27, v242
v_mfma_f32_16x16x32_bf16 v[16:19], a[168:171], a[96:99], v[16:19]
ds_read_b64_tr_b16 a[134:135], v231 offset:18432
v_exp_f32 v246, v246
v_mfma_f32_16x16x32_bf16 v[16:19], a[172:175], a[100:103], v[16:19]
ds_read_b64_tr_b16 a[136:137], v232 offset:12288
v_mfma_f32_16x16x32_bf16 v[20:23], a[152:155], a[24:27], 0
ds_read_b64_tr_b16 a[138:139], v232 offset:18432
v_exp_f32 v247, v247
v_mfma_f32_16x16x32_bf16 v[20:23], a[156:159], a[28:31], v[20:23]
v_mfma_f32_16x16x32_bf16 v[20:23], a[160:163], a[32:35], v[20:23]
ds_read_b64_tr_b16 a[140:141], v233 offset:12288
v_cvt_pk_bf16_f32 v91, v246, v247
v_mfma_f32_16x16x32_bf16 v[20:23], a[164:167], a[36:39], v[20:23]
ds_read_b64_tr_b16 a[142:143], v233 offset:18432
v_fma_f32 v244, v60, s27, v243
v_mfma_f32_16x16x32_bf16 v[20:23], a[168:171], a[104:107], v[20:23]
ds_read_b64_tr_b16 a[144:145], v230 offset:12416
v_fma_f32 v245, v61, s27, v243
v_mfma_f32_16x16x32_bf16 v[20:23], a[172:175], a[108:111], v[20:23]
v_exp_f32 v244, v244
v_mfma_f32_16x16x32_bf16 v[24:27], a[152:155], a[48:51], 0
ds_read_b64_tr_b16 a[146:147], v230 offset:18560
v_exp_f32 v245, v245
v_mfma_f32_16x16x32_bf16 v[24:27], a[156:159], a[52:55], v[24:27]
ds_read_b64_tr_b16 a[148:149], v231 offset:12416
v_mfma_f32_16x16x32_bf16 v[24:27], a[160:163], a[56:59], v[24:27]
v_cvt_pk_bf16_f32 v94, v244, v245
v_mfma_f32_16x16x32_bf16 v[24:27], a[164:167], a[60:63], v[24:27]
ds_read_b64_tr_b16 a[150:151], v231 offset:18560
v_fma_f32 v246, v62, s27, v243
v_mfma_f32_16x16x32_bf16 v[24:27], a[168:171], a[112:115], v[24:27]
v_fma_f32 v247, v63, s27, v243
v_exp_f32 v246, v246
v_mfma_f32_16x16x32_bf16 v[24:27], a[172:175], a[116:119], v[24:27]
v_mfma_f32_16x16x32_bf16 v[28:31], a[152:155], a[72:75], 0
v_exp_f32 v247, v247
v_mfma_f32_16x16x32_bf16 v[28:31], a[156:159], a[76:79], v[28:31]
v_cvt_pk_bf16_f32 v95, v246, v247
v_mfma_f32_16x16x32_bf16 v[28:31], a[160:163], a[80:83], v[28:31]
v_mfma_f32_16x16x32_bf16 v[28:31], a[164:167], a[84:87], v[28:31]
v_mfma_f32_16x16x32_bf16 v[28:31], a[168:171], a[120:123], v[28:31]
v_mfma_f32_16x16x32_bf16 v[28:31], a[172:175], a[124:127], v[28:31]
; ==== period 16
s_waitcnt lgkmcnt(0)
v_mfma_f32_16x16x32_bf16 a[176:179], a[192:195], v[80:83], a[176:179]
ds_read_b64_tr_b16 a[152:153], v232 offset:12416
s_mov_b32 s42, 0xc1c00000
v_mfma_f32_16x16x32_bf16 a[180:183], a[192:195], v[84:87], a[180:183]
ds_read_b64_tr_b16 a[154:155], v232 offset:18560
v_max3_f32 v244, v0, v1, v2
v_mfma_f32_16x16x32_bf16 a[184:187], a[192:195], v[88:91], a[184:187]
ds_read_b64_tr_b16 a[156:157], v233 offset:12416
v_max3_f32 v245, v4, v5, v6
v_mfma_f32_16x16x32_bf16 a[188:191], a[192:195], v[92:95], a[188:191]
v_max3_f32 v246, v8, v9, v10
v_max3_f32 v247, v12, v13, v14
v_mfma_f32_16x16x32_bf16 v[96:99], a[128:131], v[80:83], v[96:99]
ds_read_b64_tr_b16 a[158:159], v233 offset:18560
v_max3_f32 v244, v244, v3, v16
v_mfma_f32_16x16x32_bf16 v[100:103], a[128:131], v[84:87], v[100:103]
ds_read_b64_tr_b16 a[160:161], v230 offset:12544
v_max3_f32 v245, v245, v7, v20
v_mfma_f32_16x16x32_bf16 v[104:107], a[128:131], v[88:91], v[104:107]
ds_read_b64_tr_b16 a[162:163], v230 offset:18688
v_max3_f32 v246, v246, v11, v24
v_mfma_f32_16x16x32_bf16 v[108:111], a[128:131], v[92:95], v[108:111]
v_max3_f32 v247, v247, v15, v28
v_max3_f32 v244, v244, v17, v18
v_mfma_f32_16x16x32_bf16 v[112:115], a[132:135], v[80:83], v[112:115]
ds_read_b64_tr_b16 a[164:165], v231 offset:12544
v_max3_f32 v245, v245, v21, v22
v_mfma_f32_16x16x32_bf16 v[116:119], a[132:135], v[84:87], v[116:119]
ds_read_b64_tr_b16 a[166:167], v231 offset:18688
v_max3_f32 v246, v246, v25, v26
v_mfma_f32_16x16x32_bf16 v[120:123], a[132:135], v[88:91], v[120:123]
ds_read_b64_tr_b16 a[168:169], v232 offset:12544
v_max3_f32 v247, v247, v29, v30
v_mfma_f32_16x16x32_bf16 v[124:127], a[132:135], v[92:95], v[124:127]
v_max_f32 v244, v244, v19
v_max_f32 v245, v245, v23
v_mfma_f32_16x16x32_bf16 v[128:131], a[136:139], v[80:83], v[128:131]
ds_read_b64_tr_b16 a[170:171], v232 offset:18688
v_max_f32 v246, v246, v27
v_mfma_f32_16x16x32_bf16 v[132:135], a[136:139], v[84:87], v[132:135]
ds_read_b64_tr_b16 a[172:173], v233 offset:12544
v_max_f32 v247, v247, v31
v_mfma_f32_16x16x32_bf16 v[136:139], a[136:139], v[88:91], v[136:139]
v_mov_b32 v240, v244
v_mov_b32 v241, v245
v_mfma_f32_16x16x32_bf16 v[140:143], a[136:139], v[92:95], v[140:143]
ds_read_b64_tr_b16 a[174:175], v233 offset:18688
v_mov_b32 v242, v246
v_mfma_f32_16x16x32_bf16 v[144:147], a[140:143], v[80:83], v[144:147]
v_mov_b32 v243, v247
v_permlane32_swap_b32 v244, v240
v_mfma_f32_16x16x32_bf16 v[148:151], a[140:143], v[84:87], v[148:151]
v_permlane32_swap_b32 v245, v241
v_permlane32_swap_b32 v246, v242
v_mfma_f32_16x16x32_bf16 v[152:155], a[140:143], v[88:91], v[152:155]
v_permlane32_swap_b32 v247, v243
v_max_f32 v244, v244, v240
v_mfma_f32_16x16x32_bf16 v[156:159], a[140:143], v[92:95], v[156:159]
v_xor_b32 v230, 24576, v230
v_max_f32 v245, v245, v241
v_mfma_f32_16x16x32_bf16 v[160:163], a[144:147], v[80:83], v[160:163]
v_max_f32 v246, v246, v242
v_max_f32 v247, v247, v243
v_mfma_f32_16x16x32_bf16 v[164:167], a[144:147], v[84:87], v[164:167]
v_xor_b32 v231, 24576, v231
v_mov_b32 v240, v244
v_mfma_f32_16x16x32_bf16 v[168:171], a[144:147], v[88:91], v[168:171]
v_mov_b32 v241, v245
v_mov_b32 v242, v246
v_mfma_f32_16x16x32_bf16 v[172:175], a[144:147], v[92:95], v[172:175]
v_xor_b32 v232, 24576, v232
v_mov_b32 v243, v247
v_mfma_f32_16x16x32_bf16 v[176:179], a[148:151], v[80:83], v[176:179]
v_permlane16_swap_b32 v244, v240
v_permlane16_swap_b32 v245, v241
v_mfma_f32_16x16x32_bf16 v[180:183], a[148:151], v[84:87], v[180:183]
v_xor_b32 v233, 24576, v233
v_permlane16_swap_b32 v246, v242
v_mfma_f32_16x16x32_bf16 v[184:187], a[148:151], v[88:91], v[184:187]
v_permlane16_swap_b32 v247, v243
v_max_f32 v244, v244, v240
v_mfma_f32_16x16x32_bf16 v[188:191], a[148:151], v[92:95], v[188:191]
v_max_f32 v245, v245, v241
v_max_f32 v246, v246, v242
s_waitcnt lgkmcnt(0)
s_waitcnt vmcnt(16)
s_barrier
v_mfma_f32_16x16x32_bf16 v[192:195], a[152:155], v[80:83], v[192:195]
ds_read_b128 a[128:131], v228 offset:12288
v_max_f32 v247, v247, v243
v_mfma_f32_16x16x32_bf16 v[196:199], a[152:155], v[84:87], v[196:199]
ds_read_b128 a[132:135], v229 offset:12288
v_mov_b32 v248, s42
v_mfma_f32_16x16x32_bf16 v[200:203], a[152:155], v[88:91], v[200:203]
ds_read_b128 a[136:139], v228 offset:12416
v_fma_f32 v240, -v244, s27, v248
v_mfma_f32_16x16x32_bf16 v[204:207], a[152:155], v[92:95], v[204:207]
v_fma_f32 v241, -v245, s27, v248
v_fma_f32 v242, -v246, s27, v248
v_mfma_f32_16x16x32_bf16 v[208:211], a[156:159], v[80:83], v[208:211]
ds_read_b128 a[140:143], v229 offset:12416
v_fma_f32 v243, -v247, s27, v248
v_mfma_f32_16x16x32_bf16 v[212:215], a[156:159], v[84:87], v[212:215]
ds_read_b128 a[144:147], v228 offset:12544
v_fma_f32 v244, v0, s27, v240
v_mfma_f32_16x16x32_bf16 v[216:219], a[156:159], v[88:91], v[216:219]
v_fma_f32 v245, v1, s27, v240
v_fma_f32 v246, v2, s27, v240
v_mfma_f32_16x16x32_bf16 v[220:223], a[156:159], v[92:95], v[220:223]
ds_read_b128 a[148:151], v229 offset:12544
v_fma_f32 v247, v3, s27, v240
v_mfma_f32_16x16x32_bf16 v[224:227], a[160:163], v[80:83], v[224:227]
v_exp_f32 v244, v244
v_mfma_f32_16x16x32_bf16 a[196:199], a[160:163], v[84:87], a[196:199]
s_add_u32 m0, s48, 0
v_mfma_f32_16x16x32_bf16 a[200:203], a[160:163], v[88:91], a[200:203]
v_exp_f32 v245, v245
v_mfma_f32_16x16x32_bf16 a[204:207], a[160:163], v[92:95], a[204:207]
v_cvt_pk_bf16_f32 v64, v244, v245
v_fma_f32 v244, v4, s27, v241
v_mfma_f32_16x16x32_bf16 a[208:211], a[164:167], v[80:83], a[208:211]
buffer_load_dwordx4 v234, s[8:11], s24 offen nt lds
v_fma_f32 v245, v5, s27, v241
v_mfma_f32_16x16x32_bf16 a[212:215], a[164:167], v[84:87], a[212:215]
v_exp_f32 v246, v246
v_mfma_f32_16x16x32_bf16 a[216:219], a[164:167], v[88:91], a[216:219]
v_exp_f32 v247, v247
v_mfma_f32_16x16x32_bf16 a[220:223], a[164:167], v[92:95], a[220:223]
buffer_load_dwordx4 v235, s[8:11], s24 offen offset:1024 nt lds
v_cvt_pk_bf16_f32 v65, v246, v247
v_mfma_f32_16x16x32_bf16 a[224:227], a[168:171], v[80:83], a[224:227]
v_exp_f32 v244, v244
v_mfma_f32_16x16x32_bf16 a[228:231], a[168:171], v[84:87], a[228:231]
v_exp_f32 v245, v245
v_mfma_f32_16x16x32_bf16 a[232:235], a[168:171], v[88:91], a[232:235]
buffer_load_dwordx4 v236, s[8:11], s24 offen offset:2048 nt lds
v_cvt_pk_bf16_f32 v68, v244, v245
v_mfma_f32_16x16x32_bf16 a[236:239], a[168:171], v[92:95], a[236:239]
v_fma_f32 v246, v6, s27, v241
v_fma_f32 v247, v7, s27, v241
v_mfma_f32_16x16x32_bf16 a[240:243], a[172:175], v[80:83], a[240:243]
v_exp_f32 v246, v246
v_mfma_f32_16x16x32_bf16 a[244:247], a[172:175], v[84:87], a[244:247]
v_exp_f32 v247, v247
v_mfma_f32_16x16x32_bf16 a[248:251], a[172:175], v[88:91], a[248:251]
s_add_u32 s24, s24, 12288
v_cvt_pk_bf16_f32 v69, v246, v247
v_mfma_f32_16x16x32_bf16 a[252:255], a[172:175], v[92:95], a[252:255]
v_fma_f32 v244, v8, s27, v242
v_fma_f32 v245, v9, s27, v242
v_exp_f32 v244, v244
s_nop 0
v_exp_f32 v245, v245
s_nop 0
v_cvt_pk_bf16_f32 v72, v244, v245
s_nop 7
; ---- epilogue 0
v_mbcnt_lo_u32_b32 v247, -1, 0
v_mbcnt_hi_u32_b32 v247, -1, v247
v_and_b32 v246, 15, v247
v_mul_u32_u24 v244, 384, v246
v_add_u32 v244, s30, v244
v_and_b32 v246, 14, v246
v_lshrrev_b32 v245, 4, v247
v_xor_b32 v246, v246, v245
v_xor_b32 v250, 0, v246
v_lshl_add_u32 v250, v250, 3, v244
v_xor_b32 v251, 4, v246
v_lshl_add_u32 v251, v251, 3, v244
v_xor_b32 v252, 8, v246
v_lshl_add_u32 v252, v252, 3, v244
v_xor_b32 v253, 12, v246
v_lshl_add_u32 v253, v253, 3, v244
v_xor_b32 v249, s31, v234
v_add_u32 v249, s30, v249
v_xor_b32 v254, s31, v235
v_add_u32 v254, s30, v254
v_xor_b32 v255, s31, v236
v_add_u32 v255, s30, v255
v_lshlrev_b32 v247, 4, v247
v_accvgpr_read_b32 v244, a176
v_cmp_nge_f32 vcc, 0x5b800000, v244
v_rcp_f32 v244, v244
s_nop 1
s_or_b64 s[96:97], s[96:97], vcc
s_nop 0
v_pk_mul_f32 v[96:97], v[96:97], v[244:245] op_sel_hi:[1,0]
v_pk_mul_f32 v[98:99], v[98:99], v[244:245] op_sel_hi:[1,0]
v_pk_mul_f32 v[112:113], v[112:113], v[244:245] op_sel_hi:[1,0]
v_pk_mul_f32 v[114:115], v[114:115], v[244:245] op_sel_hi:[1,0]
v_cvt_pk_bf16_f32 v96, v96, v97
v_cvt_pk_bf16_f32 v97, v98, v99
v_cvt_pk_bf16_f32 v112, v112, v113
v_cvt_pk_bf16_f32 v113, v114, v115
ds_write_b64 v250, v[96:97] offset:0
ds_write_b64 v251, v[112:113] offset:0
v_pk_mul_f32 v[128:129], v[128:129], v[244:245] op_sel_hi:[1,0]
v_pk_mul_f32 v[130:131], v[130:131], v[244:245] op_sel_hi:[1,0]
v_pk_mul_f32 v[144:145], v[144:145], v[244:245] op_sel_hi:[1,0]
v_pk_mul_f32 v[146:147], v[146:147], v[244:245] op_sel_hi:[1,0]
v_cvt_pk_bf16_f32 v128, v128, v129
v_cvt_pk_bf16_f32 v129, v130, v131
v_cvt_pk_bf16_f32 v144, v144, v145
v_cvt_pk_bf16_f32 v145, v146, v147
ds_write_b64 v252, v[128:129] offset:0
ds_write_b64 v253, v[144:145] offset:0
v_pk_mul_f32 v[160:161], v[160:161], v[244:245] op_sel_hi:[1,0]
v_pk_mul_f32 v[162:163], v[162:163], v[244:245] op_sel_hi:[1,0]
v_pk_mul_f32 v[176:177], v[176:177], v[244:245] op_sel_hi:[1,0]
v_pk_mul_f32 v[178:179], v[178:179], v[244:245] op_sel_hi:[1,0]
v_cvt_pk_bf16_f32 v160, v160, v161
v_cvt_pk_bf16_f32 v161, v162, v163
v_cvt_pk_bf16_f32 v176, v176, v177
v_cvt_pk_bf16_f32 v177, v178, v179
ds_write_b64 v250, v[160:161] offset:128
ds_write_b64 v251, v[176:177] offset:128
v_pk_mul_f32 v[192:193], v[192:193], v[244:245] op_sel_hi:[1,0]
v_pk_mul_f32 v[194:195], v[194:195], v[244:245] op_sel_hi:[1,0]
v_pk_mul_f32 v[208:209], v[208:209], v[244:245] op_sel_hi:[1,0]
v_pk_mul_f32 v[210:211], v[210:211], v[244:245] op_sel_hi:[1,0]
v_cvt_pk_bf16_f32 v192, v192, v193
v_cvt_pk_bf16_f32 v193, v194, v195
v_cvt_pk_bf16_f32 v208, v208, v209
v_cvt_pk_bf16_f32 v209, v210, v211
ds_write_b64 v252, v[192:193] offset:128
ds_write_b64 v253, v[208:209] offset:128
v_pk_mul_f32 v[224:225], v[224:225], v[244:245] op_sel_hi:[1,0]
v_pk_mul_f32 v[226:227], v[226:227], v[244:245] op_sel_hi:[1,0]
v_cvt_pk_bf16_f32 v224, v224, v225
v_cvt_pk_bf16_f32 v225, v226, v227
ds_write_b64 v250, v[224:225] offset:256
v_accvgpr_read_b32 v192, a208
v_accvgpr_read_b32 v193, a209
v_accvgpr_read_b32 v194, a210
v_accvgpr_read_b32 v195, a211
v_accvgpr_read_b32 v176, a224
v_accvgpr_read_b32 v177, a225
v_accvgpr_read_b32 v178, a226
v_accvgpr_read_b32 v179, a227
v_accvgpr_read_b32 v160, a240
v_accvgpr_read_b32 v161, a241
v_accvgpr_read_b32 v162, a242
v_accvgpr_read_b32 v163, a243
v_pk_mul_f32 v[192:193], v[192:193], v[244:245] op_sel_hi:[1,0]
v_pk_mul_f32 v[194:195], v[194:195], v[244:245] op_sel_hi:[1,0]
v_pk_mul_f32 v[176:177], v[176:177], v[244:245] op_sel_hi:[1,0]
v_pk_mul_f32 v[178:179], v[178:179], v[244:245] op_sel_hi:[1,0]
v_pk_mul_f32 v[160:161], v[160:161], v[244:245] op_sel_hi:[1,0]
v_pk_mul_f32 v[162:163], v[162:163], v[244:245] op_sel_hi:[1,0]
v_cvt_pk_bf16_f32 v192, v192, v193
v_cvt_pk_bf16_f32 v193, v194, v195
v_cvt_pk_bf16_f32 v176, v176, v177
v_cvt_pk_bf16_f32 v177, v178, v179
v_cvt_pk_bf16_f32 v160, v160, v161
v_cvt_pk_bf16_f32 v161, v162, v163
ds_write_b64 v251, v[192:193] offset:256
ds_write_b64 v252, v[176:177] offset:256
ds_write_b64 v253, v[160:161] offset:256
ds_read_b128 v[96:99], v249 offset:0
ds_read_b128 v[112:115], v254 offset:1024
ds_read_b128 v[128:131], v255 offset:2048
v_xor_b32 v246, 64, v249
ds_read_b128 v[144:147], v246 offset:3072
v_xor_b32 v246, 64, v254
ds_read_b128 v[160:163], v246 offset:4096
v_xor_b32 v246, 64, v255
ds_read_b128 v[176:179], v246 offset:5120
v_accvgpr_read_b32 v244, a180
v_cmp_nge_f32 vcc, 0x5b800000, v244
v_rcp_f32 v244, v244
s_nop 1
s_or_b64 s[96:97], s[96:97], vcc
s_nop 0
v_pk_mul_f32 v[100:101], v[100:101], v[244:245] op_sel_hi:[1,0]
v_pk_mul_f32 v[102:103], v[102:103], v[244:245] op_sel_hi:[1,0]
v_pk_mul_f32 v[116:117], v[116:117], v[244:245] op_sel_hi:[1,0]
v_pk_mul_f32 v[118:119], v[118:119], v[244:245] op_sel_hi:[1,0]
v_cvt_pk_bf16_f32 v100, v100, v101
v_cvt_pk_bf16_f32 v101, v102, v103
v_cvt_pk_bf16_f32 v116, v116, v117
v_cvt_pk_bf16_f32 v117, v118, v119
ds_write_b64 v250, v[100:101] offset:6144
ds_write_b64 v251, v[116:117] offset:6144
v_pk_mul_f32 v[132:133], v[132:133], v[244:245] op_sel_hi:[1,0]
v_pk_mul_f32 v[134:135], v[134:135], v[244:245] op_sel_hi:[1,0]
v_pk_mul_f32 v[148:149], v[148:149], v[244:245] op_sel_hi:[1,0]
v_pk_mul_f32 v[150:151], v[150:151], v[244:245] op_sel_hi:[1,0]
v_cvt_pk_bf16_f32 v132, v132, v133
v_cvt_pk_bf16_f32 v133, v134, v135
v_cvt_pk_bf16_f32 v148, v148, v149
v_cvt_pk_bf16_f32 v149, v150, v151
ds_write_b64 v252, v[132:133] offset:6144
ds_write_b64 v253, v[148:149] offset:6144
v_pk_mul_f32 v[164:165], v[164:165], v[244:245] op_sel_hi:[1,0]
v_pk_mul_f32 v[166:167], v[166:167], v[244:245] op_sel_hi:[1,0]
v_pk_mul_f32 v[180:181], v[180:181], v[244:245] op_sel_hi:[1,0]
v_pk_mul_f32 v[182:183], v[182:183], v[244:245] op_sel_hi:[1,0]
v_cvt_pk_bf16_f32 v164, v164, v165
v_cvt_pk_bf16_f32 v165, v166, v167
v_cvt_pk_bf16_f32 v180, v180, v181
v_cvt_pk_bf16_f32 v181, v182, v183
ds_write_b64 v250, v[164:165] offset:6272
ds_write_b64 v251, v[180:181] offset:6272
v_pk_mul_f32 v[196:197], v[196:197], v[244:245] op_sel_hi:[1,0]
v_pk_mul_f32 v[198:199], v[198:199], v[244:245] op_sel_hi:[1,0]
v_pk_mul_f32 v[212:213], v[212:213], v[244:245] op_sel_hi:[1,0]
v_pk_mul_f32 v[214:215], v[214:215], v[244:245] op_sel_hi:[1,0]
v_cvt_pk_bf16_f32 v196, v196, v197
v_cvt_pk_bf16_f32 v197, v198, v199
v_cvt_pk_bf16_f32 v212, v212, v213
v_cvt_pk_bf16_f32 v213, v214, v215
ds_write_b64 v252, v[196:197] offset:6272
ds_write_b64 v253, v[212:213] offset:6272
v_accvgpr_read_b32 v180, a196
v_accvgpr_read_b32 v181, a197
v_accvgpr_read_b32 v182, a198
v_accvgpr_read_b32 v183, a199
v_accvgpr_read_b32 v164, a212
v_accvgpr_read_b32 v165, a213
v_accvgpr_read_b32 v166, a214
v_accvgpr_read_b32 v167, a215
v_accvgpr_read_b32 v148, a228
v_accvgpr_read_b32 v149, a229
v_accvgpr_read_b32 v150, a230
v_accvgpr_read_b32 v151, a231
v_accvgpr_read_b32 v132, a244
v_accvgpr_read_b32 v133, a245
v_accvgpr_read_b32 v134, a246
v_accvgpr_read_b32 v135, a247
v_pk_mul_f32 v[180:181], v[180:181], v[244:245] op_sel_hi:[1,0]
v_pk_mul_f32 v[182:183], v[182:183], v[244:245] op_sel_hi:[1,0]
v_pk_mul_f32 v[164:165], v[164:165], v[244:245] op_sel_hi:[1,0]
v_pk_mul_f32 v[166:167], v[166:167], v[244:245] op_sel_hi:[1,0]
v_pk_mul_f32 v[148:149], v[148:149], v[244:245] op_sel_hi:[1,0]
v_pk_mul_f32 v[150:151], v[150:151], v[244:245] op_sel_hi:[1,0]
v_pk_mul_f32 v[132:133], v[132:133], v[244:245] op_sel_hi:[1,0]
v_pk_mul_f32 v[134:135], v[134:135], v[244:245] op_sel_hi:[1,0]
v_cvt_pk_bf16_f32 v180, v180, v181
v_cvt_pk_bf16_f32 v181, v182, v183
v_cvt_pk_bf16_f32 v164, v164, v165
v_cvt_pk_bf16_f32 v165, v166, v167
v_cvt_pk_bf16_f32 v148, v148, v149
v_cvt_pk_bf16_f32 v149, v150, v151
v_cvt_pk_bf16_f32 v132, v132, v133
v_cvt_pk_bf16_f32 v133, v134, v135
ds_write_b64 v250, v[180:181] offset:6400
ds_write_b64 v251, v[164:165] offset:6400
ds_write_b64 v252, v[148:149] offset:6400
ds_write_b64 v253, v[132:133] offset:6400
ds_read_b128 a[16:19], v249 offset:6144
ds_read_b128 a[20:23], v254 offset:7168
ds_read_b128 a[40:43], v255 offset:8192
v_xor_b32 v246, 64, v249
ds_read_b128 a[44:47], v246 offset:9216
v_xor_b32 v246, 64, v254
ds_read_b128 a[64:67], v246 offset:10240
v_xor_b32 v246, 64, v255
ds_read_b128 a[68:71], v246 offset:11264
s_waitcnt lgkmcnt(15)
s_add_u32 s42, s26, 0
s_add_u32 s43, s26, 4096
v_accvgpr_read_b32 v244, a184
v_cmp_nge_f32 vcc, 0x5b800000, v244
v_rcp_f32 v244, v244
s_nop 1
s_or_b64 s[96:97], s[96:97], vcc
s_nop 0
v_pk_mul_f32 v[104:105], v[104:105], v[244:245] op_sel_hi:[1,0]
v_pk_mul_f32 v[106:107], v[106:107], v[244:245] op_sel_hi:[1,0]
v_pk_mul_f32 v[120:121], v[120:121], v[244:245] op_sel_hi:[1,0]
v_pk_mul_f32 v[122:123], v[122:123], v[244:245] op_sel_hi:[1,0]
v_cvt_pk_bf16_f32 v104, v104, v105
v_cvt_pk_bf16_f32 v105, v106, v107
v_cvt_pk_bf16_f32 v120, v120, v121
v_cvt_pk_bf16_f32 v121, v122, v123
ds_write_b64 v250, v[104:105] offset:0
ds_write_b64 v251, v[120:121] offset:0
v_pk_mul_f32 v[136:137], v[136:137], v[244:245] op_sel_hi:[1,0]
v_pk_mul_f32 v[138:139], v[138:139], v[244:245] op_sel_hi:[1,0]
v_pk_mul_f32 v[152:153], v[152:153], v[244:245] op_sel_hi:[1,0]
v_pk_mul_f32 v[154:155], v[154:155], v[244:245] op_sel_hi:[1,0]
v_cvt_pk_bf16_f32 v136, v136, v137
v_cvt_pk_bf16_f32 v137, v138, v139
v_cvt_pk_bf16_f32 v152, v152, v153
v_cvt_pk_bf16_f32 v153, v154, v155
ds_write_b64 v252, v[136:137] offset:0
ds_write_b64 v253, v[152:153] offset:0
v_pk_mul_f32 v[168:169], v[168:169], v[244:245] op_sel_hi:[1,0]
v_pk_mul_f32 v[170:171], v[170:171], v[244:245] op_sel_hi:[1,0]
buffer_store_dwordx4 v[96:99], v247, s[20:23], s42 offen offset:0 sc0 sc1
v_pk_mul_f32 v[184:185], v[184:185], v[244:245] op_sel_hi:[1,0]
v_pk_mul_f32 v[186:187], v[186:187], v[244:245] op_sel_hi:[1,0]
v_cvt_pk_bf16_f32 v168, v168, v169
v_cvt_pk_bf16_f32 v169, v170, v171
v_cvt_pk_bf16_f32 v184, v184, v185
v_cvt_pk_bf16_f32 v185, v186, v187
ds_write_b64 v250, v[168:169] offset:128
ds_write_b64 v251, v[184:185] offset:128
v_pk_mul_f32 v[200:201], v[200:201], v[244:245] op_sel_hi:[1,0]
v_pk_mul_f32 v[202:203], v[202:203], v[244:245] op_sel_hi:[1,0]
v_pk_mul_f32 v[216:217], v[216:217], v[244:245] op_sel_hi:[1,0]
v_pk_mul_f32 v[218:219], v[218:219], v[244:245] op_sel_hi:[1,0]
v_cvt_pk_bf16_f32 v200, v200, v201
v_cvt_pk_bf16_f32 v201, v202, v203
v_cvt_pk_bf16_f32 v216, v216, v217
v_cvt_pk_bf16_f32 v217, v218, v219
ds_write_b64 v252, v[200:201] offset:128
ds_write_b64 v253, v[216:217] offset:128
v_accvgpr_read_b32 v184, a200
v_accvgpr_read_b32 v185, a201
v_accvgpr_read_b32 v186, a202
v_accvgpr_read_b32 v187, a203
v_accvgpr_read_b32 v168, a216
buffer_store_dwordx4 v[112:115], v247, s[20:23], s42 offen offset:1024 sc0 sc1
v_accvgpr_read_b32 v169, a217
v_accvgpr_read_b32 v170, a218
v_accvgpr_read_b32 v171, a219
v_accvgpr_read_b32 v152, a232
v_accvgpr_read_b32 v153, a233
v_accvgpr_read_b32 v154, a234
v_accvgpr_read_b32 v155, a235
v_accvgpr_read_b32 v136, a248
v_accvgpr_read_b32 v137, a249
v_accvgpr_read_b32 v138, a250
v_accvgpr_read_b32 v139, a251
v_pk_mul_f32 v[184:185], v[184:185], v[244:245] op_sel_hi:[1,0]
v_pk_mul_f32 v[186:187], v[186:187], v[244:245] op_sel_hi:[1,0]
v_pk_mul_f32 v[168:169], v[168:169], v[244:245] op_sel_hi:[1,0]
v_pk_mul_f32 v[170:171], v[170:171], v[244:245] op_sel_hi:[1,0]
v_pk_mul_f32 v[152:153], v[152:153], v[244:245] op_sel_hi:[1,0]
v_pk_mul_f32 v[154:155], v[154:155], v[244:245] op_sel_hi:[1,0]
v_pk_mul_f32 v[136:137], v[136:137], v[244:245] op_sel_hi:[1,0]
v_pk_mul_f32 v[138:139], v[138:139], v[244:245] op_sel_hi:[1,0]
v_cvt_pk_bf16_f32 v184, v184, v185
v_cvt_pk_bf16_f32 v185, v186, v187
v_cvt_pk_bf16_f32 v168, v168, v169
v_cvt_pk_bf16_f32 v169, v170, v171
buffer_store_dwordx4 v[128:131], v247, s[20:23], s42 offen offset:2048 sc0 sc1
v_cvt_pk_bf16_f32 v152, v152, v153
v_cvt_pk_bf16_f32 v153, v154, v155
v_cvt_pk_bf16_f32 v136, v136, v137
v_cvt_pk_bf16_f32 v137, v138, v139
ds_write_b64 v250, v[184:185] offset:256
ds_write_b64 v251, v[168:169] offset:256
ds_write_b64 v252, v[152:153] offset:256
ds_write_b64 v253, v[136:137] offset:256
v_accvgpr_read_b32 v244, a188
v_cmp_nge_f32 vcc, 0x5b800000, v244
v_rcp_f32 v244, v244
s_nop 1
s_or_b64 s[96:97], s[96:97], vcc
s_nop 0
v_pk_mul_f32 v[108:109], v[108:109], v[244:245] op_sel_hi:[1,0]
v_pk_mul_f32 v[110:111], v[110:111], v[244:245] op_sel_hi:[1,0]
v_pk_mul_f32 v[124:125], v[124:125], v[244:245] op_sel_hi:[1,0]
v_pk_mul_f32 v[126:127], v[126:127], v[244:245] op_sel_hi:[1,0]
v_cvt_pk_bf16_f32 v108, v108, v109
v_cvt_pk_bf16_f32 v109, v110, v111
v_cvt_pk_bf16_f32 v124, v124, v125
v_cvt_pk_bf16_f32 v125, v126, v127
ds_write_b64 v250, v[108:109] offset:6144
buffer_store_dwordx4 v[144:147], v247, s[20:23], s42 offen offset:3072 sc0 sc1
ds_write_b64 v251, v[124:125] offset:6144
v_pk_mul_f32 v[140:141], v[140:141], v[244:245] op_sel_hi:[1,0]
v_pk_mul_f32 v[142:143], v[142:143], v[244:245] op_sel_hi:[1,0]
v_pk_mul_f32 v[156:157], v[156:157], v[244:245] op_sel_hi:[1,0]
v_pk_mul_f32 v[158:159], v[158:159], v[244:245] op_sel_hi:[1,0]
v_cvt_pk_bf16_f32 v140, v140, v141
v_cvt_pk_bf16_f32 v141, v142, v143
v_cvt_pk_bf16_f32 v156, v156, v157
v_cvt_pk_bf16_f32 v157, v158, v159
ds_write_b64 v252, v[140:141] offset:6144
ds_write_b64 v253, v[156:157] offset:6144
v_pk_mul_f32 v[172:173], v[172:173], v[244:245] op_sel_hi:[1,0]
v_pk_mul_f32 v[174:175], v[174:175], v[244:245] op_sel_hi:[1,0]
v_pk_mul_f32 v[188:189], v[188:189], v[244:245] op_sel_hi:[1,0]
v_pk_mul_f32 v[190:191], v[190:191], v[244:245] op_sel_hi:[1,0]
v_cvt_pk_bf16_f32 v172, v172, v173
v_cvt_pk_bf16_f32 v173, v174, v175
v_cvt_pk_bf16_f32 v188, v188, v189
v_cvt_pk_bf16_f32 v189, v190, v191
ds_write_b64 v250, v[172:173] offset:6272
ds_write_b64 v251, v[188:189] offset:6272
v_pk_mul_f32 v[204:205], v[204:205], v[244:245] op_sel_hi:[1,0]
v_pk_mul_f32 v[206:207], v[206:207], v[244:245] op_sel_hi:[1,0]
buffer_store_dwordx4 v[160:163], v247, s[20:23], s43 offen offset:0 sc0 sc1
v_pk_mul_f32 v[220:221], v[220:221], v[244:245] op_sel_hi:[1,0]
v_pk_mul_f32 v[222:223], v[222:223], v[244:245] op_sel_hi:[1,0]
v_cvt_pk_bf16_f32 v204, v204, v205
v_cvt_pk_bf16_f32 v205, v206, v207
v_cvt_pk_bf16_f32 v220, v220, v221
v_cvt_pk_bf16_f32 v221, v222, v223
ds_write_b64 v252, v[204:205] offset:6272
ds_write_b64 v253, v[220:221] offset:6272
v_accvgpr_read_b32 v188, a204
v_accvgpr_read_b32 v189, a205
v_accvgpr_read_b32 v190, a206
v_accvgpr_read_b32 v191, a207
v_accvgpr_read_b32 v172, a220
v_accvgpr_read_b32 v173, a221
v_accvgpr_read_b32 v174, a222
v_accvgpr_read_b32 v175, a223
v_accvgpr_read_b32 v156, a236
v_accvgpr_read_b32 v157, a237
v_accvgpr_read_b32 v158, a238
v_accvgpr_read_b32 v159, a239
v_accvgpr_read_b32 v140, a252
v_accvgpr_read_b32 v141, a253
v_accvgpr_read_b32 v142, a254
v_accvgpr_read_b32 v143, a255
buffer_store_dwordx4 v[176:179], v247, s[20:23], s43 offen offset:1024 sc0 sc1
v_pk_mul_f32 v[188:189], v[188:189], v[244:245] op_sel_hi:[1,0]
v_pk_mul_f32 v[190:191], v[190:191], v[244:245] op_sel_hi:[1,0]
v_pk_mul_f32 v[172:173], v[172:173], v[244:245] op_sel_hi:[1,0]
v_pk_mul_f32 v[174:175], v[174:175], v[244:245] op_sel_hi:[1,0]
v_pk_mul_f32 v[156:157], v[156:157], v[244:245] op_sel_hi:[1,0]
v_pk_mul_f32 v[158:159], v[158:159], v[244:245] op_sel_hi:[1,0]
v_pk_mul_f32 v[140:141], v[140:141], v[244:245] op_sel_hi:[1,0]
v_pk_mul_f32 v[142:143], v[142:143], v[244:245] op_sel_hi:[1,0]
v_cvt_pk_bf16_f32 v188, v188, v189
v_cvt_pk_bf16_f32 v189, v190, v191
v_cvt_pk_bf16_f32 v172, v172, v173
v_cvt_pk_bf16_f32 v173, v174, v175
v_cvt_pk_bf16_f32 v156, v156, v157
v_cvt_pk_bf16_f32 v157, v158, v159
v_cvt_pk_bf16_f32 v140, v140, v141
v_cvt_pk_bf16_f32 v141, v142, v143
ds_write_b64 v250, v[188:189] offset:6400
ds_write_b64 v251, v[172:173] offset:6400
ds_write_b64 v252, v[156:157] offset:6400
ds_write_b64 v253, v[140:141] offset:6400
v_mov_b32 v250, v247
s_add_u32 s53, s26, 10240
s_add_u32 s56, s26, 6144
s_add_u32 s57, s26, 10240
s_mov_b32 s58, 2048
s_waitcnt lgkmcnt(15)
v_mfma_f32_16x16x32_bf16 v[32:35], a[128:131], a[0:3], 0
ds_read_b128 a[152:155], v228 offset:18432
v_fma_f32 v246, v10, s27, v242
v_mfma_f32_16x16x32_bf16 v[32:35], a[132:135], a[4:7], v[32:35]
s_add_u32 m0, s49, 12288
v_fma_f32 v247, v11, s27, v242
v_mfma_f32_16x16x32_bf16 v[32:35], a[136:139], a[8:11], v[32:35]
ds_read_b128 a[156:159], v229 offset:18432
buffer_load_dwordx4 v237, s[12:15], s25 offen nt lds
v_mfma_f32_16x16x32_bf16 v[32:35], a[140:143], a[12:15], v[32:35]
v_exp_f32 v246, v246
v_mfma_f32_16x16x32_bf16 v[32:35], a[144:147], a[96:99], v[32:35]
ds_read_b128 a[160:163], v228 offset:18560
buffer_load_dwordx4 v238, s[12:15], s25 offen offset:1024 nt lds
v_mfma_f32_16x16x32_bf16 v[32:35], a[148:151], a[100:103], v[32:35]
v_exp_f32 v247, v247
v_mfma_f32_16x16x32_bf16 v[36:39], a[128:131], a[24:27], 0
ds_read_b128 a[164:167], v229 offset:18560
buffer_load_dwordx4 v239, s[12:15], s25 offen offset:2048 nt lds
v_mfma_f32_16x16x32_bf16 v[36:39], a[132:135], a[28:31], v[36:39]
v_cvt_pk_bf16_f32 v73, v246, v247
v_fma_f32 v244, v12, s27, v243
v_mfma_f32_16x16x32_bf16 v[36:39], a[136:139], a[32:35], v[36:39]
ds_read_b128 a[168:171], v228 offset:18688
s_add_u32 s25, s25, 12288
v_mfma_f32_16x16x32_bf16 v[36:39], a[140:143], a[36:39], v[36:39]
v_fma_f32 v245, v13, s27, v243
v_fma_f32 v246, v14, s27, v243
v_mfma_f32_16x16x32_bf16 v[36:39], a[144:147], a[104:107], v[36:39]
s_xor_b32 s51, s51, 24576
v_fma_f32 v247, v15, s27, v243
v_mfma_f32_16x16x32_bf16 v[36:39], a[148:151], a[108:111], v[36:39]
ds_read_b128 a[172:175], v229 offset:18688
v_mfma_f32_16x16x32_bf16 v[40:43], a[128:131], a[48:51], 0
s_add_u32 s49, s29, s51
v_mfma_f32_16x16x32_bf16 v[40:43], a[132:135], a[52:55], v[40:43]
v_xor_b32 v228, 24576, v228
v_mfma_f32_16x16x32_bf16 v[40:43], a[136:139], a[56:59], v[40:43]
buffer_store_dwordx4 a[16:19], v250, s[20:23], s56 offen offset:0 sc0 sc1
v_mfma_f32_16x16x32_bf16 v[40:43], a[140:143], a[60:63], v[40:43]
v_xor_b32 v229, 24576, v229
v_mfma_f32_16x16x32_bf16 v[40:43], a[144:147], a[112:115], v[40:43]
v_exp_f32 v244, v244
v_mfma_f32_16x16x32_bf16 v[40:43], a[148:151], a[116:119], v[40:43]
v_exp_f32 v245, v245
v_mfma_f32_16x16x32_bf16 v[44:47], a[128:131], a[72:75], 0
v_cvt_pk_bf16_f32 v76, v244, v245
v_fma_f32 v244, v16, s27, v240
v_mfma_f32_16x16x32_bf16 v[44:47], a[132:135], a[76:79], v[44:47]
buffer_store_dwordx4 a[20:23], v250, s[20:23], s56 offen offset:1024 sc0 sc1
v_fma_f32 v245, v17, s27, v240
v_mfma_f32_16x16x32_bf16 v[44:47], a[136:139], a[80:83], v[44:47]
v_exp_f32 v246, v246
v_mfma_f32_16x16x32_bf16 v[44:47], a[140:143], a[84:87], v[44:47]
v_exp_f32 v247, v247
v_mfma_f32_16x16x32_bf16 v[44:47], a[144:147], a[120:123], v[44:47]
v_cvt_pk_bf16_f32 v77, v246, v247
v_fma_f32 v246, v18, s27, v240
v_mfma_f32_16x16x32_bf16 v[44:47], a[148:151], a[124:127], v[44:47]
v_exp_f32 v244, v244
s_waitcnt lgkmcnt(0)
v_mfma_f32_16x16x32_bf16 v[48:51], a[152:155], a[0:3], 0
ds_read_b64_tr_b16 a[128:129], v230 offset:0
v_fma_f32 v247, v19, s27, v240
v_mfma_f32_16x16x32_bf16 v[48:51], a[156:159], a[4:7], v[48:51]
ds_read_b64_tr_b16 a[130:131], v230 offset:6144
v_mfma_f32_16x16x32_bf16 v[48:51], a[160:163], a[8:11], v[48:51]
ds_read_b64_tr_b16 a[132:133], v231 offset:0
v_mfma_f32_16x16x32_bf16 v[48:51], a[164:167], a[12:15], v[48:51]
v_exp_f32 v245, v245
v_mfma_f32_16x16x32_bf16 v[48:51], a[168:171], a[96:99], v[48:51]
ds_read_b64_tr_b16 a[134:135], v231 offset:6144
v_cvt_pk_bf16_f32 v66, v244, v245
v_mfma_f32_16x16x32_bf16 v[48:51], a[172:175], a[100:103], v[48:51]
ds_read_b64_tr_b16 a[136:137], v232 offset:0
v_fma_f32 v244, v20, s27, v241
v_mfma_f32_16x16x32_bf16 v[52:55], a[152:155], a[24:27], 0
ds_read_b64_tr_b16 a[138:139], v232 offset:6144
v_fma_f32 v245, v21, s27, v241
v_mfma_f32_16x16x32_bf16 v[52:55], a[156:159], a[28:31], v[52:55]
v_exp_f32 v246, v246
v_mfma_f32_16x16x32_bf16 v[52:55], a[160:163], a[32:35], v[52:55]
ds_read_b64_tr_b16 a[140:141], v233 offset:0
v_mfma_f32_16x16x32_bf16 v[52:55], a[164:167], a[36:39], v[52:55]
ds_read_b64_tr_b16 a[142:143], v233 offset:6144
v_mfma_f32_16x16x32_bf16 v[52:55], a[168:171], a[104:107], v[52:55]
ds_read_b64_tr_b16 a[144:145], v230 offset:128
v_mfma_f32_16x16x32_bf16 v[52:55], a[172:175], a[108:111], v[52:55]
v_exp_f32 v247, v247
v_mfma_f32_16x16x32_bf16 v[56:59], a[152:155], a[48:51], 0
ds_read_b64_tr_b16 a[146:147], v230 offset:6272
v_cvt_pk_bf16_f32 v67, v246, v247
v_mfma_f32_16x16x32_bf16 v[56:59], a[156:159], a[52:55], v[56:59]
ds_read_b64_tr_b16 a[148:149], v231 offset:128
v_fma_f32 v246, v22, s27, v241
v_mfma_f32_16x16x32_bf16 v[56:59], a[160:163], a[56:59], v[56:59]
v_exp_f32 v244, v244
v_mfma_f32_16x16x32_bf16 v[56:59], a[164:167], a[60:63], v[56:59]
ds_read_b64_tr_b16 a[150:151], v231 offset:6272
v_fma_f32 v247, v23, s27, v241
v_mfma_f32_16x16x32_bf16 v[56:59], a[168:171], a[112:115], v[56:59]
v_exp_f32 v245, v245
v_mfma_f32_16x16x32_bf16 v[56:59], a[172:175], a[116:119], v[56:59]
buffer_store_dwordx4 a[40:43], v250, s[20:23], s56 offen offset:2048 sc0 sc1
v_cvt_pk_bf16_f32 v70, v244, v245
v_mfma_f32_16x16x32_bf16 v[60:63], a[152:155], a[72:75], 0
v_exp_f32 v246, v246
v_mfma_f32_16x16x32_bf16 v[60:63], a[156:159], a[76:79], v[60:63]
v_exp_f32 v247, v247
v_mfma_f32_16x16x32_bf16 v[60:63], a[160:163], a[80:83], v[60:63]
v_cvt_pk_bf16_f32 v71, v246, v247
v_fma_f32 v244, v24, s27, v242
v_mfma_f32_16x16x32_bf16 v[60:63], a[164:167], a[84:87], v[60:63]
buffer_store_dwordx4 a[44:47], v250, s[20:23], s56 offen offset:3072 sc0 sc1
v_mfma_f32_16x16x32_bf16 v[60:63], a[168:171], a[120:123], v[60:63]
v_mfma_f32_16x16x32_bf16 v[60:63], a[172:175], a[124:127], v[60:63]
v_fma_f32 v245, v25, s27, v242
v_exp_f32 v244, v244
s_nop 0
v_exp_f32 v245, v245
s_nop 0
v_cvt_pk_bf16_f32 v74, v244, v245
v_fma_f32 v246, v26, s27, v242
v_fma_f32 v247, v27, s27, v242
v_exp_f32 v246, v246
s_nop 0
v_exp_f32 v247, v247
s_nop 0
v_cvt_pk_bf16_f32 v75, v246, v247
v_fma_f32 v244, v28, s27, v243
v_fma_f32 v245, v29, s27, v243
v_exp_f32 v244, v244
s_nop 0
v_exp_f32 v245, v245
s_nop 0
v_cvt_pk_bf16_f32 v78, v244, v245
v_fma_f32 v246, v30, s27, v243
v_fma_f32 v247, v31, s27, v243
v_exp_f32 v246, v246
s_nop 0
v_exp_f32 v247, v247
s_nop 0
v_cvt_pk_bf16_f32 v79, v246, v247
s_mov_b32 s52, 6
; ==== period 17 (B part)
s_waitcnt lgkmcnt(0)
v_mfma_f32_16x16x32_bf16 a[176:179], a[192:195], v[64:67], 0
ds_read_b64_tr_b16 a[152:153], v232 offset:128
v_fma_f32 v244, v32, s27, v240
v_mfma_f32_16x16x32_bf16 a[180:183], a[192:195], v[68:71], 0
ds_read_b64_tr_b16 a[154:155], v232 offset:6272
v_fma_f32 v245, v33, s27, v240
v_mfma_f32_16x16x32_bf16 a[184:187], a[192:195], v[72:75], 0
ds_read_b64_tr_b16 a[156:157], v233 offset:128
v_fma_f32 v246, v34, s27, v240
v_mfma_f32_16x16x32_bf16 a[188:191], a[192:195], v[76:79], 0
v_exp_f32 v244, v244
v_mfma_f32_16x16x32_bf16 v[96:99], a[128:131], v[64:67], 0
ds_read_b64_tr_b16 a[158:159], v233 offset:6272
v_fma_f32 v247, v35, s27, v240
v_mfma_f32_16x16x32_bf16 v[100:103], a[128:131], v[68:71], 0
ds_read_b64_tr_b16 a[160:161], v230 offset:256
v_mfma_f32_16x16x32_bf16 v[104:107], a[128:131], v[72:75], 0
ds_read_b64_tr_b16 a[162:163], v230 offset:6400
v_mfma_f32_16x16x32_bf16 v[108:111], a[128:131], v[76:79], 0
v_exp_f32 v245, v245
v_mfma_f32_16x16x32_bf16 v[112:115], a[132:135], v[64:67], 0
ds_read_b64_tr_b16 a[164:165], v231 offset:256
v_cvt_pk_bf16_f32 v80, v244, v245
v_mfma_f32_16x16x32_bf16 v[116:119], a[132:135], v[68:71], 0
ds_read_b64_tr_b16 a[166:167], v231 offset:6400
v_fma_f32 v244, v36, s27, v241
v_mfma_f32_16x16x32_bf16 v[120:123], a[132:135], v[72:75], 0
ds_read_b64_tr_b16 a[168:169], v232 offset:256
v_fma_f32 v245, v37, s27, v241
v_mfma_f32_16x16x32_bf16 v[124:127], a[132:135], v[76:79], 0
v_exp_f32 v246, v246
v_mfma_f32_16x16x32_bf16 v[128:131], a[136:139], v[64:67], 0
ds_read_b64_tr_b16 a[170:171], v232 offset:6400
v_mfma_f32_16x16x32_bf16 v[132:135], a[136:139], v[68:71], 0
ds_read_b64_tr_b16 a[172:173], v233 offset:256
v_mfma_f32_16x16x32_bf16 v[136:139], a[136:139], v[72:75], 0
v_exp_f32 v247, v247
v_mfma_f32_16x16x32_bf16 v[140:143], a[136:139], v[76:79], 0
ds_read_b64_tr_b16 a[174:175], v233 offset:6400
v_cvt_pk_bf16_f32 v81, v246, v247
v_mfma_f32_16x16x32_bf16 v[144:147], a[140:143], v[64:67], 0
v_exp_f32 v244, v244
v_mfma_f32_16x16x32_bf16 v[148:151], a[140:143], v[68:71], 0
v_exp_f32 v245, v245
v_mfma_f32_16x16x32_bf16 v[152:155], a[140:143], v[72:75], 0
v_cvt_pk_bf16_f32 v84, v244, v245
v_fma_f32 v246, v38, s27, v241
v_mfma_f32_16x16x32_bf16 v[156:159], a[140:143], v[76:79], 0
v_fma_f32 v247, v39, s27, v241
v_fma_f32 v244, v40, s27, v242
v_mfma_f32_16x16x32_bf16 v[160:163], a[144:147], v[64:67], 0
buffer_store_dwordx4 a[64:67], v250, s[20:23], s57 offen offset:0 sc0 sc1
v_exp_f32 v246, v246
v_mfma_f32_16x16x32_bf16 v[164:167], a[144:147], v[68:71], 0
v_exp_f32 v247, v247
v_mfma_f32_16x16x32_bf16 v[168:171], a[144:147], v[72:75], 0
v_mfma_f32_16x16x32_bf16 v[172:175], a[144:147], v[76:79], 0
v_cvt_pk_bf16_f32 v85, v246, v247
v_fma_f32 v245, v41, s27, v242
v_mfma_f32_16x16x32_bf16 v[176:179], a[148:151], v[64:67], 0
v_exp_f32 v244, v244
v_mfma_f32_16x16x32_bf16 v[180:183], a[148:151], v[68:71], 0
v_exp_f32 v245, v245
v_mfma_f32_16x16x32_bf16 v[184:187], a[148:151], v[72:75], 0
v_cvt_pk_bf16_f32 v88, v244, v245
v_mfma_f32_16x16x32_bf16 v[188:191], a[148:151], v[76:79], 0
v_fma_f32 v246, v42, s27, v242
s_waitcnt lgkmcnt(0)
s_waitcnt vmcnt(25)
s_barrier
v_mfma_f32_16x16x32_bf16 v[192:195], a[152:155], v[64:67], 0
ds_read_b128 a[128:131], v228 offset:0
v_fma_f32 v247, v43, s27, v242
v_mfma_f32_16x16x32_bf16 v[196:199], a[152:155], v[68:71], 0
ds_read_b128 a[132:135], v229 offset:0
v_fma_f32 v244, v44, s27, v243
v_mfma_f32_16x16x32_bf16 v[200:203], a[152:155], v[72:75], 0
ds_read_b128 a[136:139], v228 offset:128
v_fma_f32 v245, v45, s27, v243
v_mfma_f32_16x16x32_bf16 v[204:207], a[152:155], v[76:79], 0
v_exp_f32 v246, v246
v_mfma_f32_16x16x32_bf16 v[208:211], a[156:159], v[64:67], 0
ds_read_b128 a[140:143], v229 offset:128
v_mfma_f32_16x16x32_bf16 v[212:215], a[156:159], v[68:71], 0
ds_read_b128 a[144:147], v228 offset:256
v_mfma_f32_16x16x32_bf16 v[216:219], a[156:159], v[72:75], 0
v_exp_f32 v247, v247
v_mfma_f32_16x16x32_bf16 v[220:223], a[156:159], v[76:79], 0
ds_read_b128 a[148:151], v229 offset:256
v_cvt_pk_bf16_f32 v89, v246, v247
v_mfma_f32_16x16x32_bf16 v[224:227], a[160:163], v[64:67], 0
v_exp_f32 v244, v244
v_mfma_f32_16x16x32_bf16 a[196:199], a[160:163], v[68:71], 0
s_add_u32 m0, s48, 12288
v_fma_f32 v246, v46, s27, v243
v_mfma_f32_16x16x32_bf16 a[200:203], a[160:163], v[72:75], 0
v_exp_f32 v245, v245
v_mfma_f32_16x16x32_bf16 a[204:207], a[160:163], v[76:79], 0
buffer_load_dwordx4 v234, s[8:11], s24 offen nt lds
v_mfma_f32_16x16x32_bf16 a[208:211], a[164:167], v[64:67], 0
buffer_store_dwordx4 a[68:71], v250, s[20:23], s57 offen offset:1024 sc0 sc1
v_cvt_pk_bf16_f32 v92, v244, v245
v_fma_f32 v247, v47, s27, v243
v_mfma_f32_16x16x32_bf16 a[212:215], a[164:167], v[68:71], 0
buffer_load_dwordx4 v235, s[8:11], s24 offen offset:1024 nt lds
v_fma_f32 v244, v48, s27, v240
v_mfma_f32_16x16x32_bf16 a[216:219], a[164:167], v[72:75], 0
v_exp_f32 v246, v246
v_mfma_f32_16x16x32_bf16 a[220:223], a[164:167], v[76:79], 0
buffer_load_dwordx4 v236, s[8:11], s24 offen offset:2048 nt lds
v_mfma_f32_16x16x32_bf16 a[224:227], a[168:171], v[64:67], 0
v_exp_f32 v247, v247
v_mfma_f32_16x16x32_bf16 a[228:231], a[168:171], v[68:71], 0
s_add_u32 s24, s24, 12288
v_mfma_f32_16x16x32_bf16 a[232:235], a[168:171], v[72:75], 0
v_cvt_pk_bf16_f32 v93, v246, v247
v_fma_f32 v245, v49, s27, v240
v_mfma_f32_16x16x32_bf16 a[236:239], a[168:171], v[76:79], 0
s_xor_b32 s50, s50, 24576
v_fma_f32 v246, v50, s27, v240
v_mfma_f32_16x16x32_bf16 a[240:243], a[172:175], v[64:67], 0
v_exp_f32 v244, v244
v_mfma_f32_16x16x32_bf16 a[244:247], a[172:175], v[68:71], 0
v_exp_f32 v245, v245
v_mfma_f32_16x16x32_bf16 a[248:251], a[172:175], v[72:75], 0
s_add_u32 s48, s28, s50
v_mfma_f32_16x16x32_bf16 a[252:255], a[172:175], v[76:79], 0
v_cvt_pk_bf16_f32 v82, v244, v245
s_branch L2MID_%=
LOOP2_%=:
; ==== period L2 parity 1
s_waitcnt lgkmcnt(0)
v_mfma_f32_16x16x32_bf16 a[176:179], a[192:195], v[64:67], a[176:179]
ds_read_b64_tr_b16 a[152:153], v232 offset:128
v_fma_f32 v244, v32, s27, v240
v_mfma_f32_16x16x32_bf16 a[180:183], a[192:195], v[68:71], a[180:183]
ds_read_b64_tr_b16 a[154:155], v232 offset:6272
v_fma_f32 v245, v33, s27, v240
v_mfma_f32_16x16x32_bf16 a[184:187], a[192:195], v[72:75], a[184:187]
ds_read_b64_tr_b16 a[156:157], v233 offset:128
v_fma_f32 v246, v34, s27, v240
v_mfma_f32_16x16x32_bf16 a[188:191], a[192:195], v[76:79], a[188:191]
v_exp_f32 v244, v244
v_mfma_f32_16x16x32_bf16 v[96:99], a[128:131], v[64:67], v[96:99]
ds_read_b64_tr_b16 a[158:159], v233 offset:6272
v_fma_f32 v247, v35, s27, v240
v_mfma_f32_16x16x32_bf16 v[100:103], a[128:131], v[68:71], v[100:103]
ds_read_b64_tr_b16 a[160:161], v230 offset:256
v_mfma_f32_16x16x32_bf16 v[104:107], a[128:131], v[72:75], v[104:107]
ds_read_b64_tr_b16 a[162:163], v230 offset:6400
v_mfma_f32_16x16x32_bf16 v[108:111], a[128:131], v[76:79], v[108:111]
v_exp_f32 v245, v245
v_mfma_f32_16x16x32_bf16 v[112:115], a[132:135], v[64:67], v[112:115]
ds_read_b64_tr_b16 a[164:165], v231 offset:256
v_cvt_pk_bf16_f32 v80, v244, v245
v_mfma_f32_16x16x32_bf16 v[116:119], a[132:135], v[68:71], v[116:119]
ds_read_b64_tr_b16 a[166:167], v231 offset:6400
v_fma_f32 v244, v36, s27, v241
v_mfma_f32_16x16x32_bf16 v[120:123], a[132:135], v[72:75], v[120:123]
ds_read_b64_tr_b16 a[168:169], v232 offset:256
v_fma_f32 v245, v37, s27, v241
v_mfma_f32_16x16x32_bf16 v[124:127], a[132:135], v[76:79], v[124:127]
v_exp_f32 v246, v246
v_mfma_f32_16x16x32_bf16 v[128:131], a[136:139], v[64:67], v[128:131]
ds_read_b64_tr_b16 a[170:171], v232 offset:6400
v_mfma_f32_16x16x32_bf16 v[132:135], a[136:139], v[68:71], v[132:135]
ds_read_b64_tr_b16 a[172:173], v233 offset:256
v_mfma_f32_16x16x32_bf16 v[136:139], a[136:139], v[72:75], v[136:139]
v_exp_f32 v247, v247
v_mfma_f32_16x16x32_bf16 v[140:143], a[136:139], v[76:79], v[140:143]
ds_read_b64_tr_b16 a[174:175], v233 offset:6400
v_cvt_pk_bf16_f32 v81, v246, v247
v_mfma_f32_16x16x32_bf16 v[144:147], a[140:143], v[64:67], v[144:147]
v_exp_f32 v244, v244
v_mfma_f32_16x16x32_bf16 v[148:151], a[140:143], v[68:71], v[148:151]
v_exp_f32 v245, v245
v_mfma_f32_16x16x32_bf16 v[152:155], a[140:143], v[72:75], v[152:155]
v_cvt_pk_bf16_f32 v84, v244, v245
v_fma_f32 v246, v38, s27, v241
v_mfma_f32_16x16x32_bf16 v[156:159], a[140:143], v[76:79], v[156:159]
v_fma_f32 v247, v39, s27, v241
v_fma_f32 v244, v40, s27, v242
v_mfma_f32_16x16x32_bf16 v[160:163], a[144:147], v[64:67], v[160:163]
v_exp_f32 v246, v246
v_mfma_f32_16x16x32_bf16 v[164:167], a[144:147], v[68:71], v[164:167]
v_exp_f32 v247, v247
v_mfma_f32_16x16x32_bf16 v[168:171], a[144:147], v[72:75], v[168:171]
v_mfma_f32_16x16x32_bf16 v[172:175], a[144:147], v[76:79], v[172:175]
v_cvt_pk_bf16_f32 v85, v246, v247
v_fma_f32 v245, v41, s27, v242
v_mfma_f32_16x16x32_bf16 v[176:179], a[148:151], v[64:67], v[176:179]
v_exp_f32 v244, v244
v_mfma_f32_16x16x32_bf16 v[180:183], a[148:151], v[68:71], v[180:183]
v_exp_f32 v245, v245
v_mfma_f32_16x16x32_bf16 v[184:187], a[148:151], v[72:75], v[184:187]
v_cvt_pk_bf16_f32 v88, v244, v245
v_mfma_f32_16x16x32_bf16 v[188:191], a[148:151], v[76:79], v[188:191]
v_fma_f32 v246, v42, s27, v242
s_waitcnt lgkmcnt(0)
s_waitcnt vmcnt(15)
s_barrier
v_mfma_f32_16x16x32_bf16 v[192:195], a[152:155], v[64:67], v[192:195]
ds_read_b128 a[128:131], v228 offset:0
v_fma_f32 v247, v43, s27, v242
v_mfma_f32_16x16x32_bf16 v[196:199], a[152:155], v[68:71], v[196:199]
ds_read_b128 a[132:135], v229 offset:0
v_fma_f32 v244, v44, s27, v243
v_mfma_f32_16x16x32_bf16 v[200:203], a[152:155], v[72:75], v[200:203]
ds_read_b128 a[136:139], v228 offset:128
v_fma_f32 v245, v45, s27, v243
v_mfma_f32_16x16x32_bf16 v[204:207], a[152:155], v[76:79], v[204:207]
v_exp_f32 v246, v246
v_mfma_f32_16x16x32_bf16 v[208:211], a[156:159], v[64:67], v[208:211]
ds_read_b128 a[140:143], v229 offset:128
v_mfma_f32_16x16x32_bf16 v[212:215], a[156:159], v[68:71], v[212:215]
ds_read_b128 a[144:147], v228 offset:256
v_mfma_f32_16x16x32_bf16 v[216:219], a[156:159], v[72:75], v[216:219]
v_exp_f32 v247, v247
v_mfma_f32_16x16x32_bf16 v[220:223], a[156:159], v[76:79], v[220:223]
ds_read_b128 a[148:151], v229 offset:256
v_cvt_pk_bf16_f32 v89, v246, v247
v_mfma_f32_16x16x32_bf16 v[224:227], a[160:163], v[64:67], v[224:227]
v_exp_f32 v244, v244
v_mfma_f32_16x16x32_bf16 a[196:199], a[160:163], v[68:71], a[196:199]
s_add_u32 m0, s48, 12288
v_fma_f32 v246, v46, s27, v243
v_mfma_f32_16x16x32_bf16 a[200:203], a[160:163], v[72:75], a[200:203]
v_exp_f32 v245, v245
v_mfma_f32_16x16x32_bf16 a[204:207], a[160:163], v[76:79], a[204:207]
buffer_load_dwordx4 v234, s[8:11], s24 offen nt lds
v_mfma_f32_16x16x32_bf16 a[208:211], a[164:167], v[64:67], a[208:211]
v_cvt_pk_bf16_f32 v92, v244, v245
v_fma_f32 v247, v47, s27, v243
v_mfma_f32_16x16x32_bf16 a[212:215], a[164:167], v[68:71], a[212:215]
buffer_load_dwordx4 v235, s[8:11], s24 offen offset:1024 nt lds
v_fma_f32 v244, v48, s27, v240
v_mfma_f32_16x16x32_bf16 a[216:219], a[164:167], v[72:75], a[216:219]
v_exp_f32 v246, v246
v_mfma_f32_16x16x32_bf16 a[220:223], a[164:167], v[76:79], a[220:223]
buffer_load_dwordx4 v236, s[8:11], s24 offen offset:2048 nt lds
v_mfma_f32_16x16x32_bf16 a[224:227], a[168:171], v[64:67], a[224:227]
v_exp_f32 v247, v247
v_mfma_f32_16x16x32_bf16 a[228:231], a[168:171], v[68:71], a[228:231]
s_add_u32 s24, s24, 12288
v_mfma_f32_16x16x32_bf16 a[232:235], a[168:171], v[72:75], a[232:235]
v_cvt_pk_bf16_f32 v93, v246, v247
v_fma_f32 v245, v49, s27, v240
v_mfma_f32_16x16x32_bf16 a[236:239], a[168:171], v[76:79], a[236:239]
s_xor_b32 s50, s50, 24576
v_fma_f32 v246, v50, s27, v240
v_mfma_f32_16x16x32_bf16 a[240:243], a[172:175], v[64:67], a[240:243]
v_exp_f32 v244, v244
v_mfma_f32_16x16x32_bf16 a[244:247], a[172:175], v[68:71], a[244:247]
v_exp_f32 v245, v245
v_mfma_f32_16x16x32_bf16 a[248:251], a[172:175], v[72:75], a[248:251]
s_add_u32 s48, s28, s50
v_mfma_f32_16x16x32_bf16 a[252:255], a[172:175], v[76:79], a[252:255]
v_cvt_pk_bf16_f32 v82, v244, v245
L2MID_%=:
s_waitcnt lgkmcnt(0)
v_mfma_f32_16x16x32_bf16 v[0:3], a[128:131], a[0:3], 0
ds_read_b128 a[152:155], v228 offset:6144
v_fma_f32 v247, v51, s27, v240
v_mfma_f32_16x16x32_bf16 v[0:3], a[132:135], a[4:7], v[0:3]
s_add_u32 m0, s49, 0
v_fma_f32 v244, v52, s27, v241
v_mfma_f32_16x16x32_bf16 v[0:3], a[136:139], a[8:11], v[0:3]
ds_read_b128 a[156:159], v229 offset:6144
v_fma_f32 v245, v53, s27, v241
v_mfma_f32_16x16x32_bf16 v[0:3], a[140:143], a[12:15], v[0:3]
buffer_load_dwordx4 v237, s[12:15], s25 offen nt lds
v_mfma_f32_16x16x32_bf16 v[0:3], a[144:147], a[96:99], v[0:3]
ds_read_b128 a[160:163], v228 offset:6272
v_mfma_f32_16x16x32_bf16 v[0:3], a[148:151], a[100:103], v[0:3]
v_exp_f32 v246, v246
v_mfma_f32_16x16x32_bf16 v[4:7], a[128:131], a[24:27], 0
ds_read_b128 a[164:167], v229 offset:6272
buffer_load_dwordx4 v238, s[12:15], s25 offen offset:1024 nt lds
v_mfma_f32_16x16x32_bf16 v[4:7], a[132:135], a[28:31], v[4:7]
v_exp_f32 v247, v247
v_mfma_f32_16x16x32_bf16 v[4:7], a[136:139], a[32:35], v[4:7]
ds_read_b128 a[168:171], v228 offset:6400
v_cvt_pk_bf16_f32 v83, v246, v247
v_mfma_f32_16x16x32_bf16 v[4:7], a[140:143], a[36:39], v[4:7]
buffer_load_dwordx4 v239, s[12:15], s25 offen offset:2048 nt lds
v_fma_f32 v246, v54, s27, v241
v_mfma_f32_16x16x32_bf16 v[4:7], a[144:147], a[104:107], v[4:7]
v_exp_f32 v244, v244
v_mfma_f32_16x16x32_bf16 v[4:7], a[148:151], a[108:111], v[4:7]
ds_read_b128 a[172:175], v229 offset:6400
v_mfma_f32_16x16x32_bf16 v[8:11], a[128:131], a[48:51], 0
s_add_u32 s25, s25, 12288
v_fma_f32 v247, v55, s27, v241
v_mfma_f32_16x16x32_bf16 v[8:11], a[132:135], a[52:55], v[8:11]
s_cmp_eq_u32 s52, 6
s_cselect_b32 s42, s55, s53
v_mfma_f32_16x16x32_bf16 v[8:11], a[136:139], a[56:59], v[8:11]
v_exp_f32 v245, v245
v_mfma_f32_16x16x32_bf16 v[8:11], a[140:143], a[60:63], v[8:11]
v_cvt_pk_bf16_f32 v86, v244, v245
v_mfma_f32_16x16x32_bf16 v[8:11], a[144:147], a[112:115], v[8:11]
buffer_store_dwordx4 a[20:23], v250, s[20:23], s42 offen offset:1024 sc0 sc1
v_fma_f32 v244, v56, s27, v242
v_mfma_f32_16x16x32_bf16 v[8:11], a[148:151], a[116:119], v[8:11]
v_exp_f32 v246, v246
v_mfma_f32_16x16x32_bf16 v[12:15], a[128:131], a[72:75], 0
v_exp_f32 v247, v247
v_mfma_f32_16x16x32_bf16 v[12:15], a[132:135], a[76:79], v[12:15]
s_add_u32 s53, s53, 2048
v_mfma_f32_16x16x32_bf16 v[12:15], a[136:139], a[80:83], v[12:15]
v_cvt_pk_bf16_f32 v87, v246, v247
v_fma_f32 v245, v57, s27, v242
v_mfma_f32_16x16x32_bf16 v[12:15], a[140:143], a[84:87], v[12:15]
v_exp_f32 v244, v244
v_mfma_f32_16x16x32_bf16 v[12:15], a[144:147], a[120:123], v[12:15]
v_exp_f32 v245, v245
v_mfma_f32_16x16x32_bf16 v[12:15], a[148:151], a[124:127], v[12:15]
ds_read_b128 a[16:19], v249 offset:0
s_waitcnt lgkmcnt(1)
v_mfma_f32_16x16x32_bf16 v[16:19], a[152:155], a[0:3], 0
ds_read_b64_tr_b16 a[128:129], v230 offset:12288
v_mfma_f32_16x16x32_bf16 v[16:19], a[156:159], a[4:7], v[16:19]
ds_read_b64_tr_b16 a[130:131], v230 offset:18432
v_cvt_pk_bf16_f32 v90, v244, v245
v_mfma_f32_16x16x32_bf16 v[16:19], a[160:163], a[8:11], v[16:19]
ds_read_b64_tr_b16 a[132:133], v231 offset:12288
v_fma_f32 v246, v58, s27, v242
v_mfma_f32_16x16x32_bf16 v[16:19], a[164:167], a[12:15], v[16:19]
v_fma_f32 v247, v59, s27, v242
v_fma_f32 v244, v60, s27, v243
v_mfma_f32_16x16x32_bf16 v[16:19], a[168:171], a[96:99], v[16:19]
ds_read_b64_tr_b16 a[134:135], v231 offset:18432
v_mfma_f32_16x16x32_bf16 v[16:19], a[172:175], a[100:103], v[16:19]
ds_read_b64_tr_b16 a[136:137], v232 offset:12288
v_fma_f32 v245, v61, s27, v243
v_mfma_f32_16x16x32_bf16 v[20:23], a[152:155], a[24:27], 0
ds_read_b64_tr_b16 a[138:139], v232 offset:18432
v_mfma_f32_16x16x32_bf16 v[20:23], a[156:159], a[28:31], v[20:23]
v_exp_f32 v246, v246
v_mfma_f32_16x16x32_bf16 v[20:23], a[160:163], a[32:35], v[20:23]
ds_read_b64_tr_b16 a[140:141], v233 offset:12288
v_mfma_f32_16x16x32_bf16 v[20:23], a[164:167], a[36:39], v[20:23]
ds_read_b64_tr_b16 a[142:143], v233 offset:18432
v_mfma_f32_16x16x32_bf16 v[20:23], a[168:171], a[104:107], v[20:23]
ds_read_b64_tr_b16 a[144:145], v230 offset:12416
v_mfma_f32_16x16x32_bf16 v[20:23], a[172:175], a[108:111], v[20:23]
v_exp_f32 v247, v247
v_mfma_f32_16x16x32_bf16 v[24:27], a[152:155], a[48:51], 0
ds_read_b64_tr_b16 a[146:147], v230 offset:18560
v_cvt_pk_bf16_f32 v91, v246, v247
v_mfma_f32_16x16x32_bf16 v[24:27], a[156:159], a[52:55], v[24:27]
ds_read_b64_tr_b16 a[148:149], v231 offset:12416
v_fma_f32 v246, v62, s27, v243
v_mfma_f32_16x16x32_bf16 v[24:27], a[160:163], a[56:59], v[24:27]
v_exp_f32 v244, v244
v_mfma_f32_16x16x32_bf16 v[24:27], a[164:167], a[60:63], v[24:27]
ds_read_b64_tr_b16 a[150:151], v231 offset:18560
v_fma_f32 v247, v63, s27, v243
v_mfma_f32_16x16x32_bf16 v[24:27], a[168:171], a[112:115], v[24:27]
v_exp_f32 v245, v245
v_mfma_f32_16x16x32_bf16 v[24:27], a[172:175], a[116:119], v[24:27]
v_cvt_pk_bf16_f32 v94, v244, v245
v_mfma_f32_16x16x32_bf16 v[28:31], a[152:155], a[72:75], 0
v_exp_f32 v246, v246
v_mfma_f32_16x16x32_bf16 v[28:31], a[156:159], a[76:79], v[28:31]
v_exp_f32 v247, v247
v_mfma_f32_16x16x32_bf16 v[28:31], a[160:163], a[80:83], v[28:31]
v_cvt_pk_bf16_f32 v95, v246, v247
v_mfma_f32_16x16x32_bf16 v[28:31], a[164:167], a[84:87], v[28:31]
v_mfma_f32_16x16x32_bf16 v[28:31], a[168:171], a[120:123], v[28:31]
v_mfma_f32_16x16x32_bf16 v[28:31], a[172:175], a[124:127], v[28:31]
; ==== period L2 parity 0
s_waitcnt lgkmcnt(0)
v_mfma_f32_16x16x32_bf16 a[176:179], a[192:195], v[80:83], a[176:179]
ds_read_b64_tr_b16 a[152:153], v232 offset:12416
v_fma_f32 v244, v0, s27, v240
v_mfma_f32_16x16x32_bf16 a[180:183], a[192:195], v[84:87], a[180:183]
ds_read_b64_tr_b16 a[154:155], v232 offset:18560
v_fma_f32 v245, v1, s27, v240
v_mfma_f32_16x16x32_bf16 a[184:187], a[192:195], v[88:91], a[184:187]
ds_read_b64_tr_b16 a[156:157], v233 offset:12416
v_fma_f32 v246, v2, s27, v240
v_mfma_f32_16x16x32_bf16 a[188:191], a[192:195], v[92:95], a[188:191]
v_exp_f32 v244, v244
v_mfma_f32_16x16x32_bf16 v[96:99], a[128:131], v[80:83], v[96:99]
ds_read_b64_tr_b16 a[158:159], v233 offset:18560
v_fma_f32 v247, v3, s27, v240
v_mfma_f32_16x16x32_bf16 v[100:103], a[128:131], v[84:87], v[100:103]
ds_read_b64_tr_b16 a[160:161], v230 offset:12544
v_mfma_f32_16x16x32_bf16 v[104:107], a[128:131], v[88:91], v[104:107]
ds_read_b64_tr_b16 a[162:163], v230 offset:18688
v_mfma_f32_16x16x32_bf16 v[108:111], a[128:131], v[92:95], v[108:111]
v_exp_f32 v245, v245
v_mfma_f32_16x16x32_bf16 v[112:115], a[132:135], v[80:83], v[112:115]
ds_read_b64_tr_b16 a[164:165], v231 offset:12544
v_cvt_pk_bf16_f32 v64, v244, v245
v_mfma_f32_16x16x32_bf16 v[116:119], a[132:135], v[84:87], v[116:119]
ds_read_b64_tr_b16 a[166:167], v231 offset:18688
v_fma_f32 v244, v4, s27, v241
v_mfma_f32_16x16x32_bf16 v[120:123], a[132:135], v[88:91], v[120:123]
ds_read_b64_tr_b16 a[168:169], v232 offset:12544
v_fma_f32 v245, v5, s27, v241
v_mfma_f32_16x16x32_bf16 v[124:127], a[132:135], v[92:95], v[124:127]
v_exp_f32 v246, v246
v_mfma_f32_16x16x32_bf16 v[128:131], a[136:139], v[80:83], v[128:131]
ds_read_b64_tr_b16 a[170:171], v232 offset:18688
v_mfma_f32_16x16x32_bf16 v[132:135], a[136:139], v[84:87], v[132:135]
ds_read_b64_tr_b16 a[172:173], v233 offset:12544
v_mfma_f32_16x16x32_bf16 v[136:139], a[136:139], v[88:91], v[136:139]
v_exp_f32 v247, v247
v_mfma_f32_16x16x32_bf16 v[140:143], a[136:139], v[92:95], v[140:143]
ds_read_b64_tr_b16 a[174:175], v233 offset:18688
v_cvt_pk_bf16_f32 v65, v246, v247
v_mfma_f32_16x16x32_bf16 v[144:147], a[140:143], v[80:83], v[144:147]
v_exp_f32 v244, v244
v_mfma_f32_16x16x32_bf16 v[148:151], a[140:143], v[84:87], v[148:151]
v_exp_f32 v245, v245
v_mfma_f32_16x16x32_bf16 v[152:155], a[140:143], v[88:91], v[152:155]
v_cvt_pk_bf16_f32 v68, v244, v245
v_fma_f32 v246, v6, s27, v241
v_mfma_f32_16x16x32_bf16 v[156:159], a[140:143], v[92:95], v[156:159]
v_xor_b32 v230, 24576, v230
v_fma_f32 v247, v7, s27, v241
v_mfma_f32_16x16x32_bf16 v[160:163], a[144:147], v[80:83], v[160:163]
v_exp_f32 v246, v246
v_mfma_f32_16x16x32_bf16 v[164:167], a[144:147], v[84:87], v[164:167]
v_xor_b32 v231, 24576, v231
v_fma_f32 v244, v8, s27, v242
v_mfma_f32_16x16x32_bf16 v[168:171], a[144:147], v[88:91], v[168:171]
v_exp_f32 v247, v247
v_mfma_f32_16x16x32_bf16 v[172:175], a[144:147], v[92:95], v[172:175]
v_xor_b32 v232, 24576, v232
v_cvt_pk_bf16_f32 v69, v246, v247
v_mfma_f32_16x16x32_bf16 v[176:179], a[148:151], v[80:83], v[176:179]
v_fma_f32 v245, v9, s27, v242
v_fma_f32 v246, v10, s27, v242
v_mfma_f32_16x16x32_bf16 v[180:183], a[148:151], v[84:87], v[180:183]
v_xor_b32 v233, 24576, v233
v_fma_f32 v247, v11, s27, v242
v_mfma_f32_16x16x32_bf16 v[184:187], a[148:151], v[88:91], v[184:187]
v_exp_f32 v244, v244
v_mfma_f32_16x16x32_bf16 v[188:191], a[148:151], v[92:95], v[188:191]
v_exp_f32 v245, v245
s_waitcnt lgkmcnt(0)
s_waitcnt vmcnt(15)
s_barrier
v_mfma_f32_16x16x32_bf16 v[192:195], a[152:155], v[80:83], v[192:195]
ds_read_b128 a[128:131], v228 offset:12288
v_mfma_f32_16x16x32_bf16 v[196:199], a[152:155], v[84:87], v[196:199]
ds_read_b128 a[132:135], v229 offset:12288
v_cvt_pk_bf16_f32 v72, v244, v245
v_mfma_f32_16x16x32_bf16 v[200:203], a[152:155], v[88:91], v[200:203]
ds_read_b128 a[136:139], v228 offset:12416
v_fma_f32 v244, v12, s27, v243
v_mfma_f32_16x16x32_bf16 v[204:207], a[152:155], v[92:95], v[204:207]
v_exp_f32 v246, v246
v_mfma_f32_16x16x32_bf16 v[208:211], a[156:159], v[80:83], v[208:211]
ds_read_b128 a[140:143], v229 offset:12416
v_mfma_f32_16x16x32_bf16 v[212:215], a[156:159], v[84:87], v[212:215]
ds_read_b128 a[144:147], v228 offset:12544
v_fma_f32 v245, v13, s27, v243
v_mfma_f32_16x16x32_bf16 v[216:219], a[156:159], v[88:91], v[216:219]
v_exp_f32 v247, v247
v_mfma_f32_16x16x32_bf16 v[220:223], a[156:159], v[92:95], v[220:223]
ds_read_b128 a[148:151], v229 offset:12544
v_cvt_pk_bf16_f32 v73, v246, v247
v_mfma_f32_16x16x32_bf16 v[224:227], a[160:163], v[80:83], v[224:227]
v_exp_f32 v244, v244
v_mfma_f32_16x16x32_bf16 a[196:199], a[160:163], v[84:87], a[196:199]
s_add_u32 m0, s48, 0
v_mfma_f32_16x16x32_bf16 a[200:203], a[160:163], v[88:91], a[200:203]
v_exp_f32 v245, v245
v_mfma_f32_16x16x32_bf16 a[204:207], a[160:163], v[92:95], a[204:207]
v_cvt_pk_bf16_f32 v76, v244, v245
v_mfma_f32_16x16x32_bf16 a[208:211], a[164:167], v[80:83], a[208:211]
buffer_load_dwordx4 v234, s[8:11], s24 offen nt lds
v_fma_f32 v246, v14, s27, v243
v_mfma_f32_16x16x32_bf16 a[212:215], a[164:167], v[84:87], a[212:215]
v_fma_f32 v247, v15, s27, v243
v_fma_f32 v244, v16, s27, v240
v_mfma_f32_16x16x32_bf16 a[216:219], a[164:167], v[88:91], a[216:219]
v_exp_f32 v246, v246
v_mfma_f32_16x16x32_bf16 a[220:223], a[164:167], v[92:95], a[220:223]
buffer_load_dwordx4 v235, s[8:11], s24 offen offset:1024 nt lds
v_fma_f32 v245, v17, s27, v240
v_mfma_f32_16x16x32_bf16 a[224:227], a[168:171], v[80:83], a[224:227]
v_exp_f32 v247, v247
v_mfma_f32_16x16x32_bf16 a[228:231], a[168:171], v[84:87], a[228:231]
v_cvt_pk_bf16_f32 v77, v246, v247
v_mfma_f32_16x16x32_bf16 a[232:235], a[168:171], v[88:91], a[232:235]
buffer_load_dwordx4 v236, s[8:11], s24 offen offset:2048 nt lds
v_fma_f32 v246, v18, s27, v240
v_mfma_f32_16x16x32_bf16 a[236:239], a[168:171], v[92:95], a[236:239]
v_exp_f32 v244, v244
v_mfma_f32_16x16x32_bf16 a[240:243], a[172:175], v[80:83], a[240:243]
v_exp_f32 v245, v245
v_mfma_f32_16x16x32_bf16 a[244:247], a[172:175], v[84:87], a[244:247]
v_cvt_pk_bf16_f32 v66, v244, v245
v_mfma_f32_16x16x32_bf16 a[248:251], a[172:175], v[88:91], a[248:251]
s_add_u32 s24, s24, 12288
v_fma_f32 v247, v19, s27, v240
v_mfma_f32_16x16x32_bf16 a[252:255], a[172:175], v[92:95], a[252:255]
v_exp_f32 v246, v246
s_waitcnt lgkmcnt(0)
v_mfma_f32_16x16x32_bf16 v[32:35], a[128:131], a[0:3], 0
ds_read_b128 a[152:155], v228 offset:18432
v_mfma_f32_16x16x32_bf16 v[32:35], a[132:135], a[4:7], v[32:35]
s_add_u32 m0, s49, 12288
v_fma_f32 v244, v20, s27, v241
v_mfma_f32_16x16x32_bf16 v[32:35], a[136:139], a[8:11], v[32:35]
ds_read_b128 a[156:159], v229 offset:18432
buffer_load_dwordx4 v237, s[12:15], s25 offen nt lds
v_mfma_f32_16x16x32_bf16 v[32:35], a[140:143], a[12:15], v[32:35]
v_exp_f32 v247, v247
v_mfma_f32_16x16x32_bf16 v[32:35], a[144:147], a[96:99], v[32:35]
ds_read_b128 a[160:163], v228 offset:18560
buffer_load_dwordx4 v238, s[12:15], s25 offen offset:1024 nt lds
v_mfma_f32_16x16x32_bf16 v[32:35], a[148:151], a[100:103], v[32:35]
v_cvt_pk_bf16_f32 v67, v246, v247
v_mfma_f32_16x16x32_bf16 v[36:39], a[128:131], a[24:27], 0
ds_read_b128 a[164:167], v229 offset:18560
buffer_load_dwordx4 v239, s[12:15], s25 offen offset:2048 nt lds
v_mfma_f32_16x16x32_bf16 v[36:39], a[132:135], a[28:31], v[36:39]
v_fma_f32 v245, v21, s27, v241
v_fma_f32 v246, v22, s27, v241
v_mfma_f32_16x16x32_bf16 v[36:39], a[136:139], a[32:35], v[36:39]
ds_read_b128 a[168:171], v228 offset:18688
s_add_u32 s25, s25, 12288
v_mfma_f32_16x16x32_bf16 v[36:39], a[140:143], a[36:39], v[36:39]
v_exp_f32 v244, v244
v_mfma_f32_16x16x32_bf16 v[36:39], a[144:147], a[104:107], v[36:39]
s_xor_b32 s51, s51, 24576
v_fma_f32 v247, v23, s27, v241
v_mfma_f32_16x16x32_bf16 v[36:39], a[148:151], a[108:111], v[36:39]
ds_read_b128 a[172:175], v229 offset:18688
v_mfma_f32_16x16x32_bf16 v[40:43], a[128:131], a[48:51], 0
s_add_u32 s49, s29, s51
v_mfma_f32_16x16x32_bf16 v[40:43], a[132:135], a[52:55], v[40:43]
v_xor_b32 v228, 24576, v228
s_mov_b32 s42, s53
v_mfma_f32_16x16x32_bf16 v[40:43], a[136:139], a[56:59], v[40:43]
buffer_store_dwordx4 a[16:19], v250, s[20:23], s42 offen offset:0 sc0 sc1
v_mfma_f32_16x16x32_bf16 v[40:43], a[140:143], a[60:63], v[40:43]
v_xor_b32 v229, 24576, v229
v_mfma_f32_16x16x32_bf16 v[40:43], a[144:147], a[112:115], v[40:43]
ds_read_b128 a[20:23], v254 offset:1024
v_mfma_f32_16x16x32_bf16 v[40:43], a[148:151], a[116:119], v[40:43]
v_exp_f32 v245, v245
v_mfma_f32_16x16x32_bf16 v[44:47], a[128:131], a[72:75], 0
v_add_u32 v251, 2048, v255
v_cvt_pk_bf16_f32 v70, v244, v245
v_mfma_f32_16x16x32_bf16 v[44:47], a[132:135], a[76:79], v[44:47]
v_xad_u32 v255, v254, 64, s58
v_fma_f32 v244, v24, s27, v242
v_mfma_f32_16x16x32_bf16 v[44:47], a[136:139], a[80:83], v[44:47]
v_exp_f32 v246, v246
v_mfma_f32_16x16x32_bf16 v[44:47], a[140:143], a[84:87], v[44:47]
v_xad_u32 v254, v249, 64, s58
v_fma_f32 v245, v25, s27, v242
v_mfma_f32_16x16x32_bf16 v[44:47], a[144:147], a[120:123], v[44:47]
v_exp_f32 v247, v247
v_mfma_f32_16x16x32_bf16 v[44:47], a[148:151], a[124:127], v[44:47]
v_mov_b32 v249, v251
v_cvt_pk_bf16_f32 v71, v246, v247
s_waitcnt lgkmcnt(1)
v_mfma_f32_16x16x32_bf16 v[48:51], a[152:155], a[0:3], 0
ds_read_b64_tr_b16 a[128:129], v230 offset:0
v_fma_f32 v246, v26, s27, v242
v_mfma_f32_16x16x32_bf16 v[48:51], a[156:159], a[4:7], v[48:51]
ds_read_b64_tr_b16 a[130:131], v230 offset:6144
v_fma_f32 v247, v27, s27, v242
v_mfma_f32_16x16x32_bf16 v[48:51], a[160:163], a[8:11], v[48:51]
ds_read_b64_tr_b16 a[132:133], v231 offset:0
v_mfma_f32_16x16x32_bf16 v[48:51], a[164:167], a[12:15], v[48:51]
v_exp_f32 v244, v244
v_mfma_f32_16x16x32_bf16 v[48:51], a[168:171], a[96:99], v[48:51]
ds_read_b64_tr_b16 a[134:135], v231 offset:6144
v_mfma_f32_16x16x32_bf16 v[48:51], a[172:175], a[100:103], v[48:51]
ds_read_b64_tr_b16 a[136:137], v232 offset:0
v_mfma_f32_16x16x32_bf16 v[52:55], a[152:155], a[24:27], 0
ds_read_b64_tr_b16 a[138:139], v232 offset:6144
v_mfma_f32_16x16x32_bf16 v[52:55], a[156:159], a[28:31], v[52:55]
v_exp_f32 v245, v245
v_mfma_f32_16x16x32_bf16 v[52:55], a[160:163], a[32:35], v[52:55]
ds_read_b64_tr_b16 a[140:141], v233 offset:0
v_cvt_pk_bf16_f32 v74, v244, v245
v_mfma_f32_16x16x32_bf16 v[52:55], a[164:167], a[36:39], v[52:55]
ds_read_b64_tr_b16 a[142:143], v233 offset:6144
v_fma_f32 v244, v28, s27, v243
v_mfma_f32_16x16x32_bf16 v[52:55], a[168:171], a[104:107], v[52:55]
ds_read_b64_tr_b16 a[144:145], v230 offset:128
v_fma_f32 v245, v29, s27, v243
v_mfma_f32_16x16x32_bf16 v[52:55], a[172:175], a[108:111], v[52:55]
v_exp_f32 v246, v246
v_mfma_f32_16x16x32_bf16 v[56:59], a[152:155], a[48:51], 0
ds_read_b64_tr_b16 a[146:147], v230 offset:6272
v_mfma_f32_16x16x32_bf16 v[56:59], a[156:159], a[52:55], v[56:59]
ds_read_b64_tr_b16 a[148:149], v231 offset:128
v_mfma_f32_16x16x32_bf16 v[56:59], a[160:163], a[56:59], v[56:59]
v_exp_f32 v247, v247
v_mfma_f32_16x16x32_bf16 v[56:59], a[164:167], a[60:63], v[56:59]
ds_read_b64_tr_b16 a[150:151], v231 offset:6272
v_cvt_pk_bf16_f32 v75, v246, v247
v_mfma_f32_16x16x32_bf16 v[56:59], a[168:171], a[112:115], v[56:59]
v_exp_f32 v244, v244
v_mfma_f32_16x16x32_bf16 v[56:59], a[172:175], a[116:119], v[56:59]
v_exp_f32 v245, v245
v_mfma_f32_16x16x32_bf16 v[60:63], a[152:155], a[72:75], 0
v_cvt_pk_bf16_f32 v78, v244, v245
v_fma_f32 v246, v30, s27, v243
v_mfma_f32_16x16x32_bf16 v[60:63], a[156:159], a[76:79], v[60:63]
v_fma_f32 v247, v31, s27, v243
v_mfma_f32_16x16x32_bf16 v[60:63], a[160:163], a[80:83], v[60:63]
v_exp_f32 v246, v246
v_mfma_f32_16x16x32_bf16 v[60:63], a[164:167], a[84:87], v[60:63]
v_mfma_f32_16x16x32_bf16 v[60:63], a[168:171], a[120:123], v[60:63]
v_mfma_f32_16x16x32_bf16 v[60:63], a[172:175], a[124:127], v[60:63]
v_exp_f32 v247, v247
s_nop 0
v_cvt_pk_bf16_f32 v79, v246, v247
s_sub_u32 s52, s52, 1
s_cmp_lg_u32 s52, 0
s_cbranch_scc1 LOOP2_%=
; ==== pass (0, 1)
s_waitcnt lgkmcnt(0)
v_mfma_f32_16x16x32_bf16 a[176:179], a[192:195], v[64:67], a[176:179]
ds_read_b64_tr_b16 a[152:153], v232 offset:128
ds_read_b64_tr_b16 a[154:155], v232 offset:6272
v_fma_f32 v244, v32, s27, v240
v_mfma_f32_16x16x32_bf16 a[180:183], a[192:195], v[68:71], a[180:183]
ds_read_b64_tr_b16 a[156:157], v233 offset:128
v_fma_f32 v245, v33, s27, v240
v_fma_f32 v246, v34, s27, v240
v_mfma_f32_16x16x32_bf16 v[96:99], a[128:131], v[64:67], v[96:99]
ds_read_b64_tr_b16 a[158:159], v233 offset:6272
ds_read_b64_tr_b16 a[160:161], v230 offset:256
v_fma_f32 v247, v35, s27, v240
v_mfma_f32_16x16x32_bf16 v[100:103], a[128:131], v[68:71], v[100:103]
ds_read_b64_tr_b16 a[162:163], v230 offset:6400
v_exp_f32 v244, v244
v_mfma_f32_16x16x32_bf16 v[112:115], a[132:135], v[64:67], v[112:115]
ds_read_b64_tr_b16 a[164:165], v231 offset:256
v_mfma_f32_16x16x32_bf16 v[116:119], a[132:135], v[68:71], v[116:119]
ds_read_b64_tr_b16 a[166:167], v231 offset:6400
ds_read_b64_tr_b16 a[168:169], v232 offset:256
v_mfma_f32_16x16x32_bf16 v[128:131], a[136:139], v[64:67], v[128:131]
ds_read_b64_tr_b16 a[170:171], v232 offset:6400
v_exp_f32 v245, v245
v_mfma_f32_16x16x32_bf16 v[132:135], a[136:139], v[68:71], v[132:135]
ds_read_b64_tr_b16 a[172:173], v233 offset:256
v_mfma_f32_16x16x32_bf16 v[144:147], a[140:143], v[64:67], v[144:147]
ds_read_b64_tr_b16 a[174:175], v233 offset:6400
v_cvt_pk_bf16_f32 v80, v244, v245
v_mfma_f32_16x16x32_bf16 v[148:151], a[140:143], v[68:71], v[148:151]
v_exp_f32 v246, v246
v_mfma_f32_16x16x32_bf16 v[160:163], a[144:147], v[64:67], v[160:163]
v_exp_f32 v247, v247
v_mfma_f32_16x16x32_bf16 v[164:167], a[144:147], v[68:71], v[164:167]
v_cvt_pk_bf16_f32 v81, v246, v247
v_mfma_f32_16x16x32_bf16 v[176:179], a[148:151], v[64:67], v[176:179]
v_fma_f32 v244, v36, s27, v241
v_mfma_f32_16x16x32_bf16 v[180:183], a[148:151], v[68:71], v[180:183]
v_fma_f32 v245, v37, s27, v241
v_exp_f32 v244, v244
s_waitcnt lgkmcnt(0)
s_waitcnt vmcnt(15)
s_barrier
v_mfma_f32_16x16x32_bf16 v[192:195], a[152:155], v[64:67], v[192:195]
ds_read_b128 a[128:131], v228 offset:0
v_mfma_f32_16x16x32_bf16 v[196:199], a[152:155], v[68:71], v[196:199]
ds_read_b128 a[132:135], v229 offset:0
v_exp_f32 v245, v245
v_mfma_f32_16x16x32_bf16 v[208:211], a[156:159], v[64:67], v[208:211]
ds_read_b128 a[136:139], v228 offset:128
v_mfma_f32_16x16x32_bf16 v[212:215], a[156:159], v[68:71], v[212:215]
ds_read_b128 a[140:143], v229 offset:128
v_cvt_pk_bf16_f32 v84, v244, v245
v_mfma_f32_16x16x32_bf16 v[224:227], a[160:163], v[64:67], v[224:227]
ds_read_b128 a[144:147], v228 offset:256
v_fma_f32 v246, v38, s27, v241
v_mfma_f32_16x16x32_bf16 a[196:199], a[160:163], v[68:71], a[196:199]
ds_read_b128 a[148:151], v229 offset:256
v_fma_f32 v247, v39, s27, v241
v_mfma_f32_16x16x32_bf16 a[208:211], a[164:167], v[64:67], a[208:211]
v_exp_f32 v246, v246
v_mfma_f32_16x16x32_bf16 a[212:215], a[164:167], v[68:71], a[212:215]
v_exp_f32 v247, v247
v_mfma_f32_16x16x32_bf16 a[224:227], a[168:171], v[64:67], a[224:227]
v_cvt_pk_bf16_f32 v85, v246, v247
v_mfma_f32_16x16x32_bf16 a[228:231], a[168:171], v[68:71], a[228:231]
v_fma_f32 v244, v48, s27, v240
v_mfma_f32_16x16x32_bf16 a[240:243], a[172:175], v[64:67], a[240:243]
v_fma_f32 v245, v49, s27, v240
v_exp_f32 v244, v244
v_mfma_f32_16x16x32_bf16 a[244:247], a[172:175], v[68:71], a[244:247]
s_waitcnt lgkmcnt(0)
v_mfma_f32_16x16x32_bf16 v[0:3], a[128:131], a[0:3], 0
ds_read_b128 a[152:155], v228 offset:6144
v_exp_f32 v245, v245
v_mfma_f32_16x16x32_bf16 v[0:3], a[132:135], a[4:7], v[0:3]
ds_read_b128 a[156:159], v229 offset:6144
v_mfma_f32_16x16x32_bf16 v[0:3], a[136:139], a[8:11], v[0:3]
ds_read_b128 a[160:163], v228 offset:6272
v_cvt_pk_bf16_f32 v82, v244, v245
v_mfma_f32_16x16x32_bf16 v[0:3], a[140:143], a[12:15], v[0:3]
ds_read_b128 a[164:167], v229 offset:6272
v_fma_f32 v246, v50, s27, v240
v_mfma_f32_16x16x32_bf16 v[0:3], a[144:147], a[96:99], v[0:3]
ds_read_b128 a[168:171], v228 offset:6400
v_fma_f32 v247, v51, s27, v240
v_mfma_f32_16x16x32_bf16 v[0:3], a[148:151], a[100:103], v[0:3]
v_exp_f32 v246, v246
v_mfma_f32_16x16x32_bf16 v[4:7], a[128:131], a[24:27], 0
ds_read_b128 a[172:175], v229 offset:6400
v_exp_f32 v247, v247
v_mfma_f32_16x16x32_bf16 v[4:7], a[132:135], a[28:31], v[4:7]
v_cvt_pk_bf16_f32 v83, v246, v247
v_mfma_f32_16x16x32_bf16 v[4:7], a[136:139], a[32:35], v[4:7]
v_fma_f32 v244, v52, s27, v241
v_mfma_f32_16x16x32_bf16 v[4:7], a[140:143], a[36:39], v[4:7]
s_mov_b32 s42, s53
v_fma_f32 v245, v53, s27, v241
v_mfma_f32_16x16x32_bf16 v[4:7], a[144:147], a[104:107], v[4:7]
v_exp_f32 v244, v244
v_mfma_f32_16x16x32_bf16 v[4:7], a[148:151], a[108:111], v[4:7]
buffer_store_dwordx4 a[20:23], v250, s[20:23], s42 offen offset:1024 sc0 sc1
v_exp_f32 v245, v245
s_waitcnt lgkmcnt(0)
v_mfma_f32_16x16x32_bf16 v[16:19], a[152:155], a[0:3], 0
ds_read_b64_tr_b16 a[128:129], v230 offset:12288
ds_read_b64_tr_b16 a[130:131], v230 offset:18432
v_mfma_f32_16x16x32_bf16 v[16:19], a[156:159], a[4:7], v[16:19]
ds_read_b64_tr_b16 a[132:133], v231 offset:12288
v_mfma_f32_16x16x32_bf16 v[16:19], a[160:163], a[8:11], v[16:19]
ds_read_b64_tr_b16 a[134:135], v231 offset:18432
ds_read_b64_tr_b16 a[136:137], v232 offset:12288
v_cvt_pk_bf16_f32 v86, v244, v245
v_mfma_f32_16x16x32_bf16 v[16:19], a[164:167], a[12:15], v[16:19]
ds_read_b64_tr_b16 a[138:139], v232 offset:18432
v_fma_f32 v246, v54, s27, v241
v_mfma_f32_16x16x32_bf16 v[16:19], a[168:171], a[96:99], v[16:19]
ds_read_b64_tr_b16 a[140:141], v233 offset:12288
v_fma_f32 v247, v55, s27, v241
v_mfma_f32_16x16x32_bf16 v[16:19], a[172:175], a[100:103], v[16:19]
ds_read_b64_tr_b16 a[142:143], v233 offset:18432
ds_read_b64_tr_b16 a[144:145], v230 offset:12416
v_mfma_f32_16x16x32_bf16 v[20:23], a[152:155], a[24:27], 0
ds_read_b64_tr_b16 a[146:147], v230 offset:18560
v_exp_f32 v246, v246
v_mfma_f32_16x16x32_bf16 v[20:23], a[156:159], a[28:31], v[20:23]
ds_read_b64_tr_b16 a[148:149], v231 offset:12416
v_mfma_f32_16x16x32_bf16 v[20:23], a[160:163], a[32:35], v[20:23]
ds_read_b64_tr_b16 a[150:151], v231 offset:18560
v_exp_f32 v247, v247
v_mfma_f32_16x16x32_bf16 v[20:23], a[164:167], a[36:39], v[20:23]
v_cvt_pk_bf16_f32 v87, v246, v247
v_mfma_f32_16x16x32_bf16 v[20:23], a[168:171], a[104:107], v[20:23]
v_mfma_f32_16x16x32_bf16 v[20:23], a[172:175], a[108:111], v[20:23]
s_waitcnt lgkmcnt(0)
v_mfma_f32_16x16x32_bf16 a[176:179], a[192:195], v[80:83], a[176:179]
ds_read_b64_tr_b16 a[152:153], v232 offset:12416
ds_read_b64_tr_b16 a[154:155], v232 offset:18560
v_fma_f32 v244, v0, s27, v240
v_mfma_f32_16x16x32_bf16 a[180:183], a[192:195], v[84:87], a[180:183]
ds_read_b64_tr_b16 a[156:157], v233 offset:12416
v_fma_f32 v245, v1, s27, v240
v_fma_f32 v246, v2, s27, v240
v_mfma_f32_16x16x32_bf16 v[96:99], a[128:131], v[80:83], v[96:99]
ds_read_b64_tr_b16 a[158:159], v233 offset:18560
ds_read_b64_tr_b16 a[160:161], v230 offset:12544
v_fma_f32 v247, v3, s27, v240
v_mfma_f32_16x16x32_bf16 v[100:103], a[128:131], v[84:87], v[100:103]
ds_read_b64_tr_b16 a[162:163], v230 offset:18688
v_exp_f32 v244, v244
v_mfma_f32_16x16x32_bf16 v[112:115], a[132:135], v[80:83], v[112:115]
ds_read_b64_tr_b16 a[164:165], v231 offset:12544
v_mfma_f32_16x16x32_bf16 v[116:119], a[132:135], v[84:87], v[116:119]
ds_read_b64_tr_b16 a[166:167], v231 offset:18688
ds_read_b64_tr_b16 a[168:169], v232 offset:12544
v_mfma_f32_16x16x32_bf16 v[128:131], a[136:139], v[80:83], v[128:131]
ds_read_b64_tr_b16 a[170:171], v232 offset:18688
v_exp_f32 v245, v245
v_mfma_f32_16x16x32_bf16 v[132:135], a[136:139], v[84:87], v[132:135]
ds_read_b64_tr_b16 a[172:173], v233 offset:12544
v_mfma_f32_16x16x32_bf16 v[144:147], a[140:143], v[80:83], v[144:147]
ds_read_b64_tr_b16 a[174:175], v233 offset:18688
v_cvt_pk_bf16_f32 v64, v244, v245
v_mfma_f32_16x16x32_bf16 v[148:151], a[140:143], v[84:87], v[148:151]
v_xor_b32 v230, 24576, v230
v_exp_f32 v246, v246
v_mfma_f32_16x16x32_bf16 v[160:163], a[144:147], v[80:83], v[160:163]
v_xor_b32 v231, 24576, v231
v_exp_f32 v247, v247
v_mfma_f32_16x16x32_bf16 v[164:167], a[144:147], v[84:87], v[164:167]
v_xor_b32 v232, 24576, v232
v_mfma_f32_16x16x32_bf16 v[176:179], a[148:151], v[80:83], v[176:179]
v_xor_b32 v233, 24576, v233
v_cvt_pk_bf16_f32 v65, v246, v247
v_mfma_f32_16x16x32_bf16 v[180:183], a[148:151], v[84:87], v[180:183]
v_fma_f32 v244, v4, s27, v241
s_waitcnt lgkmcnt(0)
s_waitcnt vmcnt(9)
s_barrier
v_mfma_f32_16x16x32_bf16 v[192:195], a[152:155], v[80:83], v[192:195]
ds_read_b128 a[128:131], v228 offset:12288
v_fma_f32 v245, v5, s27, v241
v_mfma_f32_16x16x32_bf16 v[196:199], a[152:155], v[84:87], v[196:199]
ds_read_b128 a[132:135], v229 offset:12288
v_exp_f32 v244, v244
v_mfma_f32_16x16x32_bf16 v[208:211], a[156:159], v[80:83], v[208:211]
ds_read_b128 a[136:139], v228 offset:12416
v_exp_f32 v245, v245
v_mfma_f32_16x16x32_bf16 v[212:215], a[156:159], v[84:87], v[212:215]
ds_read_b128 a[140:143], v229 offset:12416
v_mfma_f32_16x16x32_bf16 v[224:227], a[160:163], v[80:83], v[224:227]
ds_read_b128 a[144:147], v228 offset:12544
v_cvt_pk_bf16_f32 v68, v244, v245
v_mfma_f32_16x16x32_bf16 a[196:199], a[160:163], v[84:87], a[196:199]
ds_read_b128 a[148:151], v229 offset:12544
v_fma_f32 v246, v6, s27, v241
v_mfma_f32_16x16x32_bf16 a[208:211], a[164:167], v[80:83], a[208:211]
v_fma_f32 v247, v7, s27, v241
v_mfma_f32_16x16x32_bf16 a[212:215], a[164:167], v[84:87], a[212:215]
v_exp_f32 v246, v246
v_mfma_f32_16x16x32_bf16 a[224:227], a[168:171], v[80:83], a[224:227]
v_exp_f32 v247, v247
v_mfma_f32_16x16x32_bf16 a[228:231], a[168:171], v[84:87], a[228:231]
v_cvt_pk_bf16_f32 v69, v246, v247
v_mfma_f32_16x16x32_bf16 a[240:243], a[172:175], v[80:83], a[240:243]
v_fma_f32 v244, v16, s27, v240
v_fma_f32 v245, v17, s27, v240
v_mfma_f32_16x16x32_bf16 a[244:247], a[172:175], v[84:87], a[244:247]
v_exp_f32 v244, v244
s_waitcnt lgkmcnt(0)
v_mfma_f32_16x16x32_bf16 v[32:35], a[128:131], a[0:3], 0
ds_read_b128 a[152:155], v228 offset:18432
v_mfma_f32_16x16x32_bf16 v[32:35], a[132:135], a[4:7], v[32:35]
ds_read_b128 a[156:159], v229 offset:18432
v_exp_f32 v245, v245
v_mfma_f32_16x16x32_bf16 v[32:35], a[136:139], a[8:11], v[32:35]
ds_read_b128 a[160:163], v228 offset:18560
v_cvt_pk_bf16_f32 v66, v244, v245
v_mfma_f32_16x16x32_bf16 v[32:35], a[140:143], a[12:15], v[32:35]
ds_read_b128 a[164:167], v229 offset:18560
v_fma_f32 v246, v18, s27, v240
v_mfma_f32_16x16x32_bf16 v[32:35], a[144:147], a[96:99], v[32:35]
ds_read_b128 a[168:171], v228 offset:18688
v_fma_f32 v247, v19, s27, v240
v_mfma_f32_16x16x32_bf16 v[32:35], a[148:151], a[100:103], v[32:35]
v_exp_f32 v246, v246
v_mfma_f32_16x16x32_bf16 v[36:39], a[128:131], a[24:27], 0
ds_read_b128 a[172:175], v229 offset:18688
v_mfma_f32_16x16x32_bf16 v[36:39], a[132:135], a[28:31], v[36:39]
v_exp_f32 v247, v247
v_mfma_f32_16x16x32_bf16 v[36:39], a[136:139], a[32:35], v[36:39]
v_xor_b32 v228, 24576, v228
v_cvt_pk_bf16_f32 v67, v246, v247
v_mfma_f32_16x16x32_bf16 v[36:39], a[140:143], a[36:39], v[36:39]
v_fma_f32 v244, v20, s27, v241
v_fma_f32 v245, v21, s27, v241
v_mfma_f32_16x16x32_bf16 v[36:39], a[144:147], a[104:107], v[36:39]
v_xor_b32 v229, 24576, v229
v_exp_f32 v244, v244
v_mfma_f32_16x16x32_bf16 v[36:39], a[148:151], a[108:111], v[36:39]
s_waitcnt lgkmcnt(0)
v_mfma_f32_16x16x32_bf16 v[48:51], a[152:155], a[0:3], 0
ds_read_b64_tr_b16 a[128:129], v230 offset:0
ds_read_b64_tr_b16 a[130:131], v230 offset:6144
v_fma_f32 v246, v22, s27, v241
v_mfma_f32_16x16x32_bf16 v[48:51], a[156:159], a[4:7], v[48:51]
ds_read_b64_tr_b16 a[132:133], v231 offset:0
v_exp_f32 v245, v245
v_mfma_f32_16x16x32_bf16 v[48:51], a[160:163], a[8:11], v[48:51]
ds_read_b64_tr_b16 a[134:135], v231 offset:6144
ds_read_b64_tr_b16 a[136:137], v232 offset:0
v_mfma_f32_16x16x32_bf16 v[48:51], a[164:167], a[12:15], v[48:51]
ds_read_b64_tr_b16 a[138:139], v232 offset:6144
v_mfma_f32_16x16x32_bf16 v[48:51], a[168:171], a[96:99], v[48:51]
ds_read_b64_tr_b16 a[140:141], v233 offset:0
v_cvt_pk_bf16_f32 v70, v244, v245
v_fma_f32 v247, v23, s27, v241
v_mfma_f32_16x16x32_bf16 v[48:51], a[172:175], a[100:103], v[48:51]
ds_read_b64_tr_b16 a[142:143], v233 offset:6144
ds_read_b64_tr_b16 a[144:145], v230 offset:128
v_mfma_f32_16x16x32_bf16 v[52:55], a[152:155], a[24:27], 0
ds_read_b64_tr_b16 a[146:147], v230 offset:6272
v_exp_f32 v246, v246
v_mfma_f32_16x16x32_bf16 v[52:55], a[156:159], a[28:31], v[52:55]
ds_read_b64_tr_b16 a[148:149], v231 offset:128
v_mfma_f32_16x16x32_bf16 v[52:55], a[160:163], a[32:35], v[52:55]
ds_read_b64_tr_b16 a[150:151], v231 offset:6272
v_exp_f32 v247, v247
v_mfma_f32_16x16x32_bf16 v[52:55], a[164:167], a[36:39], v[52:55]
v_cvt_pk_bf16_f32 v71, v246, v247
v_mfma_f32_16x16x32_bf16 v[52:55], a[168:171], a[104:107], v[52:55]
v_mfma_f32_16x16x32_bf16 v[52:55], a[172:175], a[108:111], v[52:55]
s_waitcnt lgkmcnt(0)
v_mfma_f32_16x16x32_bf16 a[176:179], a[192:195], v[64:67], a[176:179]
ds_read_b64_tr_b16 a[152:153], v232 offset:128
ds_read_b64_tr_b16 a[154:155], v232 offset:6272
v_fma_f32 v244, v32, s27, v240
v_fma_f32 v245, v33, s27, v240
v_mfma_f32_16x16x32_bf16 a[180:183], a[192:195], v[68:71], a[180:183]
ds_read_b64_tr_b16 a[156:157], v233 offset:128
v_exp_f32 v244, v244
v_fma_f32 v246, v34, s27, v240
v_mfma_f32_16x16x32_bf16 v[96:99], a[128:131], v[64:67], v[96:99]
ds_read_b64_tr_b16 a[158:159], v233 offset:6272
ds_read_b64_tr_b16 a[160:161], v230 offset:256
v_exp_f32 v245, v245
v_mfma_f32_16x16x32_bf16 v[100:103], a[128:131], v[68:71], v[100:103]
ds_read_b64_tr_b16 a[162:163], v230 offset:6400
v_cvt_pk_bf16_f32 v80, v244, v245
v_fma_f32 v247, v35, s27, v240
v_mfma_f32_16x16x32_bf16 v[112:115], a[132:135], v[64:67], v[112:115]
ds_read_b64_tr_b16 a[164:165], v231 offset:256
v_exp_f32 v246, v246
v_mfma_f32_16x16x32_bf16 v[116:119], a[132:135], v[68:71], v[116:119]
ds_read_b64_tr_b16 a[166:167], v231 offset:6400
ds_read_b64_tr_b16 a[168:169], v232 offset:256
v_exp_f32 v247, v247
v_mfma_f32_16x16x32_bf16 v[128:131], a[136:139], v[64:67], v[128:131]
ds_read_b64_tr_b16 a[170:171], v232 offset:6400
v_cvt_pk_bf16_f32 v81, v246, v247
v_fma_f32 v244, v36, s27, v241
v_mfma_f32_16x16x32_bf16 v[132:135], a[136:139], v[68:71], v[132:135]
ds_read_b64_tr_b16 a[172:173], v233 offset:256
v_fma_f32 v245, v37, s27, v241
v_exp_f32 v244, v244
v_mfma_f32_16x16x32_bf16 v[144:147], a[140:143], v[64:67], v[144:147]
ds_read_b64_tr_b16 a[174:175], v233 offset:6400
v_exp_f32 v245, v245
v_mfma_f32_16x16x32_bf16 v[148:151], a[140:143], v[68:71], v[148:151]
v_cvt_pk_bf16_f32 v84, v244, v245
v_fma_f32 v246, v38, s27, v241
v_mfma_f32_16x16x32_bf16 v[160:163], a[144:147], v[64:67], v[160:163]
v_fma_f32 v247, v39, s27, v241
v_exp_f32 v246, v246
v_mfma_f32_16x16x32_bf16 v[164:167], a[144:147], v[68:71], v[164:167]
v_exp_f32 v247, v247
v_fma_f32 v244, v48, s27, v240
v_mfma_f32_16x16x32_bf16 v[176:179], a[148:151], v[64:67], v[176:179]
v_cvt_pk_bf16_f32 v85, v246, v247
v_fma_f32 v245, v49, s27, v240
v_exp_f32 v244, v244
v_mfma_f32_16x16x32_bf16 v[180:183], a[148:151], v[68:71], v[180:183]
v_exp_f32 v245, v245
s_waitcnt lgkmcnt(0)
s_waitcnt vmcnt(2)
s_barrier
v_mfma_f32_16x16x32_bf16 v[192:195], a[152:155], v[64:67], v[192:195]
ds_read_b64_tr_b16 a[128:129], v230 offset:12288
ds_read_b64_tr_b16 a[130:131], v230 offset:18432
v_cvt_pk_bf16_f32 v82, v244, v245
v_mfma_f32_16x16x32_bf16 v[196:199], a[152:155], v[68:71], v[196:199]
ds_read_b64_tr_b16 a[132:133], v231 offset:12288
v_fma_f32 v246, v50, s27, v240
v_fma_f32 v247, v51, s27, v240
v_fma_f32 v244, v52, s27, v241
v_mfma_f32_16x16x32_bf16 v[208:211], a[156:159], v[64:67], v[208:211]
ds_read_b64_tr_b16 a[134:135], v231 offset:18432
ds_read_b64_tr_b16 a[136:137], v232 offset:12288
v_exp_f32 v246, v246
v_mfma_f32_16x16x32_bf16 v[212:215], a[156:159], v[68:71], v[212:215]
ds_read_b64_tr_b16 a[138:139], v232 offset:18432
v_exp_f32 v247, v247
v_mfma_f32_16x16x32_bf16 v[224:227], a[160:163], v[64:67], v[224:227]
ds_read_b64_tr_b16 a[140:141], v233 offset:12288
v_cvt_pk_bf16_f32 v83, v246, v247
v_mfma_f32_16x16x32_bf16 a[196:199], a[160:163], v[68:71], a[196:199]
ds_read_b64_tr_b16 a[142:143], v233 offset:18432
ds_read_b64_tr_b16 a[144:145], v230 offset:12416
v_fma_f32 v245, v53, s27, v241
v_fma_f32 v246, v54, s27, v241
v_mfma_f32_16x16x32_bf16 a[208:211], a[164:167], v[64:67], a[208:211]
ds_read_b64_tr_b16 a[146:147], v230 offset:18560
v_exp_f32 v244, v244
v_mfma_f32_16x16x32_bf16 a[212:215], a[164:167], v[68:71], a[212:215]
ds_read_b64_tr_b16 a[148:149], v231 offset:12416
v_exp_f32 v245, v245
v_mfma_f32_16x16x32_bf16 a[224:227], a[168:171], v[64:67], a[224:227]
ds_read_b64_tr_b16 a[150:151], v231 offset:18560
v_cvt_pk_bf16_f32 v86, v244, v245
v_fma_f32 v247, v55, s27, v241
v_mfma_f32_16x16x32_bf16 a[228:231], a[168:171], v[68:71], a[228:231]
v_exp_f32 v246, v246
v_exp_f32 v247, v247
v_mfma_f32_16x16x32_bf16 a[240:243], a[172:175], v[64:67], a[240:243]
v_cvt_pk_bf16_f32 v87, v246, v247
v_mfma_f32_16x16x32_bf16 a[244:247], a[172:175], v[68:71], a[244:247]
s_waitcnt lgkmcnt(0)
v_mfma_f32_16x16x32_bf16 a[176:179], a[192:195], v[80:83], a[176:179]
ds_read_b64_tr_b16 a[152:153], v232 offset:12416
ds_read_b64_tr_b16 a[154:155], v232 offset:18560
v_mfma_f32_16x16x32_bf16 a[180:183], a[192:195], v[84:87], a[180:183]
ds_read_b64_tr_b16 a[156:157], v233 offset:12416
v_mfma_f32_16x16x32_bf16 v[96:99], a[128:131], v[80:83], v[96:99]
ds_read_b64_tr_b16 a[158:159], v233 offset:18560
ds_read_b64_tr_b16 a[160:161], v230 offset:12544
v_mfma_f32_16x16x32_bf16 v[100:103], a[128:131], v[84:87], v[100:103]
ds_read_b64_tr_b16 a[162:163], v230 offset:18688
v_mfma_f32_16x16x32_bf16 v[112:115], a[132:135], v[80:83], v[112:115]
ds_read_b64_tr_b16 a[164:165], v231 offset:12544
v_mfma_f32_16x16x32_bf16 v[116:119], a[132:135], v[84:87], v[116:119]
ds_read_b64_tr_b16 a[166:167], v231 offset:18688
ds_read_b64_tr_b16 a[168:169], v232 offset:12544
v_mfma_f32_16x16x32_bf16 v[128:131], a[136:139], v[80:83], v[128:131]
ds_read_b64_tr_b16 a[170:171], v232 offset:18688
v_mfma_f32_16x16x32_bf16 v[132:135], a[136:139], v[84:87], v[132:135]
ds_read_b64_tr_b16 a[172:173], v233 offset:12544
v_mfma_f32_16x16x32_bf16 v[144:147], a[140:143], v[80:83], v[144:147]
ds_read_b64_tr_b16 a[174:175], v233 offset:18688
v_mfma_f32_16x16x32_bf16 v[148:151], a[140:143], v[84:87], v[148:151]
v_mfma_f32_16x16x32_bf16 v[160:163], a[144:147], v[80:83], v[160:163]
v_mfma_f32_16x16x32_bf16 v[164:167], a[144:147], v[84:87], v[164:167]
v_mfma_f32_16x16x32_bf16 v[176:179], a[148:151], v[80:83], v[176:179]
v_mfma_f32_16x16x32_bf16 v[180:183], a[148:151], v[84:87], v[180:183]
s_waitcnt lgkmcnt(0)
v_mfma_f32_16x16x32_bf16 v[192:195], a[152:155], v[80:83], v[192:195]
v_mfma_f32_16x16x32_bf16 v[196:199], a[152:155], v[84:87], v[196:199]
v_mfma_f32_16x16x32_bf16 v[208:211], a[156:159], v[80:83], v[208:211]
v_mfma_f32_16x16x32_bf16 v[212:215], a[156:159], v[84:87], v[212:215]
v_mfma_f32_16x16x32_bf16 v[224:227], a[160:163], v[80:83], v[224:227]
v_mfma_f32_16x16x32_bf16 a[196:199], a[160:163], v[84:87], a[196:199]
v_mfma_f32_16x16x32_bf16 a[208:211], a[164:167], v[80:83], a[208:211]
v_mfma_f32_16x16x32_bf16 a[212:215], a[164:167], v[84:87], a[212:215]
v_mfma_f32_16x16x32_bf16 a[224:227], a[168:171], v[80:83], a[224:227]
v_mfma_f32_16x16x32_bf16 a[228:231], a[168:171], v[84:87], a[228:231]
v_mfma_f32_16x16x32_bf16 a[240:243], a[172:175], v[80:83], a[240:243]
v_mfma_f32_16x16x32_bf16 a[244:247], a[172:175], v[84:87], a[244:247]
s_nop 7
; ---- epilogue 1
v_mbcnt_lo_u32_b32 v247, -1, 0
v_mbcnt_hi_u32_b32 v247, -1, v247
v_and_b32 v246, 15, v247
v_mul_u32_u24 v244, 384, v246
v_add_u32 v244, s30, v244
v_and_b32 v246, 14, v246
v_lshrrev_b32 v245, 4, v247
v_xor_b32 v246, v246, v245
v_xor_b32 v250, 0, v246
v_lshl_add_u32 v250, v250, 3, v244
v_xor_b32 v251, 4, v246
v_lshl_add_u32 v251, v251, 3, v244
v_xor_b32 v252, 8, v246
v_lshl_add_u32 v252, v252, 3, v244
v_xor_b32 v253, 12, v246
v_lshl_add_u32 v253, v253, 3, v244
v_xor_b32 v249, s31, v234
v_add_u32 v249, s30, v249
v_xor_b32 v254, s31, v235
v_add_u32 v254, s30, v254
v_xor_b32 v255, s31, v236
v_add_u32 v255, s30, v255
v_lshlrev_b32 v247, 4, v247
v_accvgpr_read_b32 v244, a176
v_cmp_nge_f32 vcc, 0x5b800000, v244
v_rcp_f32 v244, v244
s_nop 1
s_or_b64 s[98:99], s[98:99], vcc
s_nop 0
v_pk_mul_f32 v[96:97], v[96:97], v[244:245] op_sel_hi:[1,0]
v_pk_mul_f32 v[98:99], v[98:99], v[244:245] op_sel_hi:[1,0]
v_pk_mul_f32 v[112:113], v[112:113], v[244:245] op_sel_hi:[1,0]
v_pk_mul_f32 v[114:115], v[114:115], v[244:245] op_sel_hi:[1,0]
v_cvt_pk_bf16_f32 v96, v96, v97
v_cvt_pk_bf16_f32 v97, v98, v99
v_cvt_pk_bf16_f32 v112, v112, v113
v_cvt_pk_bf16_f32 v113, v114, v115
ds_write_b64 v250, v[96:97] offset:0
ds_write_b64 v251, v[112:113] offset:0
v_pk_mul_f32 v[128:129], v[128:129], v[244:245] op_sel_hi:[1,0]
v_pk_mul_f32 v[130:131], v[130:131], v[244:245] op_sel_hi:[1,0]
v_pk_mul_f32 v[144:145], v[144:145], v[244:245] op_sel_hi:[1,0]
v_pk_mul_f32 v[146:147], v[146:147], v[244:245] op_sel_hi:[1,0]
v_cvt_pk_bf16_f32 v128, v128, v129
v_cvt_pk_bf16_f32 v129, v130, v131
v_cvt_pk_bf16_f32 v144, v144, v145
v_cvt_pk_bf16_f32 v145, v146, v147
ds_write_b64 v252, v[128:129] offset:0
ds_write_b64 v253, v[144:145] offset:0
v_pk_mul_f32 v[160:161], v[160:161], v[244:245] op_sel_hi:[1,0]
v_pk_mul_f32 v[162:163], v[162:163], v[244:245] op_sel_hi:[1,0]
v_pk_mul_f32 v[176:177], v[176:177], v[244:245] op_sel_hi:[1,0]
v_pk_mul_f32 v[178:179], v[178:179], v[244:245] op_sel_hi:[1,0]
v_cvt_pk_bf16_f32 v160, v160, v161
v_cvt_pk_bf16_f32 v161, v162, v163
v_cvt_pk_bf16_f32 v176, v176, v177
v_cvt_pk_bf16_f32 v177, v178, v179
ds_write_b64 v250, v[160:161] offset:128
ds_write_b64 v251, v[176:177] offset:128
v_pk_mul_f32 v[192:193], v[192:193], v[244:245] op_sel_hi:[1,0]
v_pk_mul_f32 v[194:195], v[194:195], v[244:245] op_sel_hi:[1,0]
v_pk_mul_f32 v[208:209], v[208:209], v[244:245] op_sel_hi:[1,0]
v_pk_mul_f32 v[210:211], v[210:211], v[244:245] op_sel_hi:[1,0]
v_cvt_pk_bf16_f32 v192, v192, v193
v_cvt_pk_bf16_f32 v193, v194, v195
v_cvt_pk_bf16_f32 v208, v208, v209
v_cvt_pk_bf16_f32 v209, v210, v211
ds_write_b64 v252, v[192:193] offset:128
ds_write_b64 v253, v[208:209] offset:128
v_pk_mul_f32 v[224:225], v[224:225], v[244:245] op_sel_hi:[1,0]
v_pk_mul_f32 v[226:227], v[226:227], v[244:245] op_sel_hi:[1,0]
v_cvt_pk_bf16_f32 v224, v224, v225
v_cvt_pk_bf16_f32 v225, v226, v227
ds_write_b64 v250, v[224:225] offset:256
v_accvgpr_read_b32 v192, a208
v_accvgpr_read_b32 v193, a209
v_accvgpr_read_b32 v194, a210
v_accvgpr_read_b32 v195, a211
v_accvgpr_read_b32 v176, a224
v_accvgpr_read_b32 v177, a225
v_accvgpr_read_b32 v178, a226
v_accvgpr_read_b32 v179, a227
v_accvgpr_read_b32 v160, a240
v_accvgpr_read_b32 v161, a241
v_accvgpr_read_b32 v162, a242
v_accvgpr_read_b32 v163, a243
v_pk_mul_f32 v[192:193], v[192:193], v[244:245] op_sel_hi:[1,0]
v_pk_mul_f32 v[194:195], v[194:195], v[244:245] op_sel_hi:[1,0]
v_pk_mul_f32 v[176:177], v[176:177], v[244:245] op_sel_hi:[1,0]
v_pk_mul_f32 v[178:179], v[178:179], v[244:245] op_sel_hi:[1,0]
v_pk_mul_f32 v[160:161], v[160:161], v[244:245] op_sel_hi:[1,0]
v_pk_mul_f32 v[162:163], v[162:163], v[244:245] op_sel_hi:[1,0]
v_cvt_pk_bf16_f32 v192, v192, v193
v_cvt_pk_bf16_f32 v193, v194, v195
v_cvt_pk_bf16_f32 v176, v176, v177
v_cvt_pk_bf16_f32 v177, v178, v179
v_cvt_pk_bf16_f32 v160, v160, v161
v_cvt_pk_bf16_f32 v161, v162, v163
ds_write_b64 v251, v[192:193] offset:256
ds_write_b64 v252, v[176:177] offset:256
ds_write_b64 v253, v[160:161] offset:256
ds_read_b128 v[96:99], v249 offset:0
ds_read_b128 v[112:115], v254 offset:1024
ds_read_b128 v[128:131], v255 offset:2048
v_xor_b32 v246, 64, v249
ds_read_b128 v[144:147], v246 offset:3072
v_xor_b32 v246, 64, v254
ds_read_b128 v[160:163], v246 offset:4096
v_xor_b32 v246, 64, v255
ds_read_b128 v[176:179], v246 offset:5120
v_accvgpr_read_b32 v244, a180
v_cmp_nge_f32 vcc, 0x5b800000, v244
v_rcp_f32 v244, v244
s_nop 1
s_or_b64 s[98:99], s[98:99], vcc
s_nop 0
v_pk_mul_f32 v[100:101], v[100:101], v[244:245] op_sel_hi:[1,0]
v_pk_mul_f32 v[102:103], v[102:103], v[244:245] op_sel_hi:[1,0]
v_pk_mul_f32 v[116:117], v[116:117], v[244:245] op_sel_hi:[1,0]
v_pk_mul_f32 v[118:119], v[118:119], v[244:245] op_sel_hi:[1,0]
v_cvt_pk_bf16_f32 v100, v100, v101
v_cvt_pk_bf16_f32 v101, v102, v103
v_cvt_pk_bf16_f32 v116, v116, v117
v_cvt_pk_bf16_f32 v117, v118, v119
ds_write_b64 v250, v[100:101] offset:6144
ds_write_b64 v251, v[116:117] offset:6144
v_pk_mul_f32 v[132:133], v[132:133], v[244:245] op_sel_hi:[1,0]
v_pk_mul_f32 v[134:135], v[134:135], v[244:245] op_sel_hi:[1,0]
v_pk_mul_f32 v[148:149], v[148:149], v[244:245] op_sel_hi:[1,0]
v_pk_mul_f32 v[150:151], v[150:151], v[244:245] op_sel_hi:[1,0]
v_cvt_pk_bf16_f32 v132, v132, v133
v_cvt_pk_bf16_f32 v133, v134, v135
v_cvt_pk_bf16_f32 v148, v148, v149
v_cvt_pk_bf16_f32 v149, v150, v151
ds_write_b64 v252, v[132:133] offset:6144
ds_write_b64 v253, v[148:149] offset:6144
v_pk_mul_f32 v[164:165], v[164:165], v[244:245] op_sel_hi:[1,0]
v_pk_mul_f32 v[166:167], v[166:167], v[244:245] op_sel_hi:[1,0]
v_pk_mul_f32 v[180:181], v[180:181], v[244:245] op_sel_hi:[1,0]
v_pk_mul_f32 v[182:183], v[182:183], v[244:245] op_sel_hi:[1,0]
v_cvt_pk_bf16_f32 v164, v164, v165
v_cvt_pk_bf16_f32 v165, v166, v167
v_cvt_pk_bf16_f32 v180, v180, v181
v_cvt_pk_bf16_f32 v181, v182, v183
ds_write_b64 v250, v[164:165] offset:6272
ds_write_b64 v251, v[180:181] offset:6272
v_pk_mul_f32 v[196:197], v[196:197], v[244:245] op_sel_hi:[1,0]
v_pk_mul_f32 v[198:199], v[198:199], v[244:245] op_sel_hi:[1,0]
v_pk_mul_f32 v[212:213], v[212:213], v[244:245] op_sel_hi:[1,0]
v_pk_mul_f32 v[214:215], v[214:215], v[244:245] op_sel_hi:[1,0]
v_cvt_pk_bf16_f32 v196, v196, v197
v_cvt_pk_bf16_f32 v197, v198, v199
v_cvt_pk_bf16_f32 v212, v212, v213
v_cvt_pk_bf16_f32 v213, v214, v215
ds_write_b64 v252, v[196:197] offset:6272
ds_write_b64 v253, v[212:213] offset:6272
v_accvgpr_read_b32 v180, a196
v_accvgpr_read_b32 v181, a197
v_accvgpr_read_b32 v182, a198
v_accvgpr_read_b32 v183, a199
v_accvgpr_read_b32 v164, a212
v_accvgpr_read_b32 v165, a213
v_accvgpr_read_b32 v166, a214
v_accvgpr_read_b32 v167, a215
v_accvgpr_read_b32 v148, a228
v_accvgpr_read_b32 v149, a229
v_accvgpr_read_b32 v150, a230
v_accvgpr_read_b32 v151, a231
v_accvgpr_read_b32 v132, a244
v_accvgpr_read_b32 v133, a245
v_accvgpr_read_b32 v134, a246
v_accvgpr_read_b32 v135, a247
v_pk_mul_f32 v[180:181], v[180:181], v[244:245] op_sel_hi:[1,0]
v_pk_mul_f32 v[182:183], v[182:183], v[244:245] op_sel_hi:[1,0]
v_pk_mul_f32 v[164:165], v[164:165], v[244:245] op_sel_hi:[1,0]
v_pk_mul_f32 v[166:167], v[166:167], v[244:245] op_sel_hi:[1,0]
v_pk_mul_f32 v[148:149], v[148:149], v[244:245] op_sel_hi:[1,0]
v_pk_mul_f32 v[150:151], v[150:151], v[244:245] op_sel_hi:[1,0]
v_pk_mul_f32 v[132:133], v[132:133], v[244:245] op_sel_hi:[1,0]
v_pk_mul_f32 v[134:135], v[134:135], v[244:245] op_sel_hi:[1,0]
v_cvt_pk_bf16_f32 v180, v180, v181
v_cvt_pk_bf16_f32 v181, v182, v183
v_cvt_pk_bf16_f32 v164, v164, v165
v_cvt_pk_bf16_f32 v165, v166, v167
v_cvt_pk_bf16_f32 v148, v148, v149
v_cvt_pk_bf16_f32 v149, v150, v151
v_cvt_pk_bf16_f32 v132, v132, v133
v_cvt_pk_bf16_f32 v133, v134, v135
ds_write_b64 v250, v[180:181] offset:6400
ds_write_b64 v251, v[164:165] offset:6400
ds_write_b64 v252, v[148:149] offset:6400
ds_write_b64 v253, v[132:133] offset:6400
ds_read_b128 v[100:103], v249 offset:6144
ds_read_b128 v[116:119], v254 offset:7168
ds_read_b128 v[132:135], v255 offset:8192
v_xor_b32 v246, 64, v249
ds_read_b128 v[148:151], v246 offset:9216
v_xor_b32 v246, 64, v254
ds_read_b128 v[164:167], v246 offset:10240
v_xor_b32 v246, 64, v255
ds_read_b128 v[180:183], v246 offset:11264
v_mov_b32 v255, v247
; ==== pass (2, 3)
v_xor_b32 v228, 24576, v228
v_xor_b32 v229, 24576, v229
v_xor_b32 v230, 24576, v230
v_xor_b32 v231, 24576, v231
v_xor_b32 v232, 24576, v232
v_xor_b32 v233, 24576, v233
ds_read_b64_tr_b16 a[128:129], v230 offset:0
ds_read_b64_tr_b16 a[130:131], v230 offset:6144
ds_read_b64_tr_b16 a[132:133], v231 offset:0
ds_read_b64_tr_b16 a[134:135], v231 offset:6144
ds_read_b64_tr_b16 a[136:137], v232 offset:0
ds_read_b64_tr_b16 a[138:139], v232 offset:6144
ds_read_b64_tr_b16 a[140:141], v233 offset:0
ds_read_b64_tr_b16 a[142:143], v233 offset:6144
ds_read_b64_tr_b16 a[144:145], v230 offset:128
ds_read_b64_tr_b16 a[146:147], v230 offset:6272
ds_read_b64_tr_b16 a[148:149], v231 offset:128
ds_read_b64_tr_b16 a[150:151], v231 offset:6272
s_waitcnt lgkmcnt(0)
v_mfma_f32_16x16x32_bf16 a[184:187], a[192:195], v[72:75], a[184:187]
ds_read_b64_tr_b16 a[152:153], v232 offset:128
ds_read_b64_tr_b16 a[154:155], v232 offset:6272
v_fma_f32 v244, v40, s27, v242
v_mfma_f32_16x16x32_bf16 a[188:191], a[192:195], v[76:79], a[188:191]
ds_read_b64_tr_b16 a[156:157], v233 offset:128
v_fma_f32 v245, v41, s27, v242
v_fma_f32 v246, v42, s27, v242
v_mfma_f32_16x16x32_bf16 v[104:107], a[128:131], v[72:75], v[104:107]
ds_read_b64_tr_b16 a[158:159], v233 offset:6272
ds_read_b64_tr_b16 a[160:161], v230 offset:256
v_fma_f32 v247, v43, s27, v242
v_mfma_f32_16x16x32_bf16 v[108:111], a[128:131], v[76:79], v[108:111]
ds_read_b64_tr_b16 a[162:163], v230 offset:6400
v_exp_f32 v244, v244
v_mfma_f32_16x16x32_bf16 v[120:123], a[132:135], v[72:75], v[120:123]
ds_read_b64_tr_b16 a[164:165], v231 offset:256
v_mfma_f32_16x16x32_bf16 v[124:127], a[132:135], v[76:79], v[124:127]
ds_read_b64_tr_b16 a[166:167], v231 offset:6400
ds_read_b64_tr_b16 a[168:169], v232 offset:256
s_add_u32 s56, s26, 196608
buffer_store_dwordx4 v[96:99], v255, s[20:23], s56 offen offset:0 sc0 sc1
v_mfma_f32_16x16x32_bf16 v[136:139], a[136:139], v[72:75], v[136:139]
ds_read_b64_tr_b16 a[170:171], v232 offset:6400
v_exp_f32 v245, v245
v_mfma_f32_16x16x32_bf16 v[140:143], a[136:139], v[76:79], v[140:143]
ds_read_b64_tr_b16 a[172:173], v233 offset:256
v_mfma_f32_16x16x32_bf16 v[152:155], a[140:143], v[72:75], v[152:155]
ds_read_b64_tr_b16 a[174:175], v233 offset:6400
v_cvt_pk_bf16_f32 v88, v244, v245
v_mfma_f32_16x16x32_bf16 v[156:159], a[140:143], v[76:79], v[156:159]
v_exp_f32 v246, v246
v_mfma_f32_16x16x32_bf16 v[168:171], a[144:147], v[72:75], v[168:171]
v_exp_f32 v247, v247
v_mfma_f32_16x16x32_bf16 v[172:175], a[144:147], v[76:79], v[172:175]
v_cvt_pk_bf16_f32 v89, v246, v247
v_mfma_f32_16x16x32_bf16 v[184:187], a[148:151], v[72:75], v[184:187]
v_fma_f32 v244, v44, s27, v243
v_mfma_f32_16x16x32_bf16 v[188:191], a[148:151], v[76:79], v[188:191]
v_fma_f32 v245, v45, s27, v243
v_exp_f32 v244, v244
s_waitcnt lgkmcnt(0)
v_mfma_f32_16x16x32_bf16 v[200:203], a[152:155], v[72:75], v[200:203]
ds_read_b128 a[128:131], v228 offset:0
v_mfma_f32_16x16x32_bf16 v[204:207], a[152:155], v[76:79], v[204:207]
ds_read_b128 a[132:135], v229 offset:0
v_exp_f32 v245, v245
v_mfma_f32_16x16x32_bf16 v[216:219], a[156:159], v[72:75], v[216:219]
ds_read_b128 a[136:139], v228 offset:128
v_mfma_f32_16x16x32_bf16 v[220:223], a[156:159], v[76:79], v[220:223]
ds_read_b128 a[140:143], v229 offset:128
v_cvt_pk_bf16_f32 v92, v244, v245
v_mfma_f32_16x16x32_bf16 a[200:203], a[160:163], v[72:75], a[200:203]
ds_read_b128 a[144:147], v228 offset:256
v_fma_f32 v246, v46, s27, v243
v_mfma_f32_16x16x32_bf16 a[204:207], a[160:163], v[76:79], a[204:207]
ds_read_b128 a[148:151], v229 offset:256
v_fma_f32 v247, v47, s27, v243
v_mfma_f32_16x16x32_bf16 a[216:219], a[164:167], v[72:75], a[216:219]
v_exp_f32 v246, v246
v_mfma_f32_16x16x32_bf16 a[220:223], a[164:167], v[76:79], a[220:223]
v_exp_f32 v247, v247
v_mfma_f32_16x16x32_bf16 a[232:235], a[168:171], v[72:75], a[232:235]
v_cvt_pk_bf16_f32 v93, v246, v247
v_mfma_f32_16x16x32_bf16 a[236:239], a[168:171], v[76:79], a[236:239]
buffer_store_dwordx4 v[112:115], v255, s[20:23], s56 offen offset:1024 sc0 sc1
v_fma_f32 v244, v56, s27, v242
v_mfma_f32_16x16x32_bf16 a[248:251], a[172:175], v[72:75], a[248:251]
v_fma_f32 v245, v57, s27, v242
v_mfma_f32_16x16x32_bf16 a[252:255], a[172:175], v[76:79], a[252:255]
v_exp_f32 v244, v244
s_waitcnt lgkmcnt(0)
v_mfma_f32_16x16x32_bf16 v[8:11], a[128:131], a[48:51], 0
ds_read_b128 a[152:155], v228 offset:6144
v_exp_f32 v245, v245
v_mfma_f32_16x16x32_bf16 v[8:11], a[132:135], a[52:55], v[8:11]
ds_read_b128 a[156:159], v229 offset:6144
v_mfma_f32_16x16x32_bf16 v[8:11], a[136:139], a[56:59], v[8:11]
ds_read_b128 a[160:163], v228 offset:6272
v_cvt_pk_bf16_f32 v90, v244, v245
v_mfma_f32_16x16x32_bf16 v[8:11], a[140:143], a[60:63], v[8:11]
ds_read_b128 a[164:167], v229 offset:6272
v_fma_f32 v246, v58, s27, v242
v_mfma_f32_16x16x32_bf16 v[8:11], a[144:147], a[112:115], v[8:11]
ds_read_b128 a[168:171], v228 offset:6400
v_fma_f32 v247, v59, s27, v242
v_mfma_f32_16x16x32_bf16 v[8:11], a[148:151], a[116:119], v[8:11]
v_exp_f32 v246, v246
v_mfma_f32_16x16x32_bf16 v[12:15], a[128:131], a[72:75], 0
ds_read_b128 a[172:175], v229 offset:6400
v_exp_f32 v247, v247
v_mfma_f32_16x16x32_bf16 v[12:15], a[132:135], a[76:79], v[12:15]
v_mfma_f32_16x16x32_bf16 v[12:15], a[136:139], a[80:83], v[12:15]
v_cvt_pk_bf16_f32 v91, v246, v247
v_fma_f32 v244, v60, s27, v243
v_mfma_f32_16x16x32_bf16 v[12:15], a[140:143], a[84:87], v[12:15]
buffer_store_dwordx4 v[128:131], v255, s[20:23], s56 offen offset:2048 sc0 sc1
v_fma_f32 v245, v61, s27, v243
v_mfma_f32_16x16x32_bf16 v[12:15], a[144:147], a[120:123], v[12:15]
v_exp_f32 v244, v244
v_mfma_f32_16x16x32_bf16 v[12:15], a[148:151], a[124:127], v[12:15]
v_exp_f32 v245, v245
s_waitcnt lgkmcnt(0)
v_mfma_f32_16x16x32_bf16 v[24:27], a[152:155], a[48:51], 0
ds_read_b64_tr_b16 a[128:129], v230 offset:12288
ds_read_b64_tr_b16 a[130:131], v230 offset:18432
v_mfma_f32_16x16x32_bf16 v[24:27], a[156:159], a[52:55], v[24:27]
ds_read_b64_tr_b16 a[132:133], v231 offset:12288
v_cvt_pk_bf16_f32 v94, v244, v245
v_mfma_f32_16x16x32_bf16 v[24:27], a[160:163], a[56:59], v[24:27]
ds_read_b64_tr_b16 a[134:135], v231 offset:18432
ds_read_b64_tr_b16 a[136:137], v232 offset:12288
v_mfma_f32_16x16x32_bf16 v[24:27], a[164:167], a[60:63], v[24:27]
ds_read_b64_tr_b16 a[138:139], v232 offset:18432
v_fma_f32 v246, v62, s27, v243
v_mfma_f32_16x16x32_bf16 v[24:27], a[168:171], a[112:115], v[24:27]
ds_read_b64_tr_b16 a[140:141], v233 offset:12288
v_fma_f32 v247, v63, s27, v243
v_mfma_f32_16x16x32_bf16 v[24:27], a[172:175], a[116:119], v[24:27]
ds_read_b64_tr_b16 a[142:143], v233 offset:18432
ds_read_b64_tr_b16 a[144:145], v230 offset:12416
v_mfma_f32_16x16x32_bf16 v[28:31], a[152:155], a[72:75], 0
ds_read_b64_tr_b16 a[146:147], v230 offset:18560
v_exp_f32 v246, v246
v_mfma_f32_16x16x32_bf16 v[28:31], a[156:159], a[76:79], v[28:31]
ds_read_b64_tr_b16 a[148:149], v231 offset:12416
v_exp_f32 v247, v247
v_mfma_f32_16x16x32_bf16 v[28:31], a[160:163], a[80:83], v[28:31]
ds_read_b64_tr_b16 a[150:151], v231 offset:18560
v_mfma_f32_16x16x32_bf16 v[28:31], a[164:167], a[84:87], v[28:31]
v_cvt_pk_bf16_f32 v95, v246, v247
v_mfma_f32_16x16x32_bf16 v[28:31], a[168:171], a[120:123], v[28:31]
buffer_store_dwordx4 v[144:147], v255, s[20:23], s56 offen offset:3072 sc0 sc1
v_mfma_f32_16x16x32_bf16 v[28:31], a[172:175], a[124:127], v[28:31]
s_waitcnt lgkmcnt(0)
v_mfma_f32_16x16x32_bf16 a[184:187], a[192:195], v[88:91], a[184:187]
ds_read_b64_tr_b16 a[152:153], v232 offset:12416
ds_read_b64_tr_b16 a[154:155], v232 offset:18560
v_fma_f32 v244, v8, s27, v242
v_mfma_f32_16x16x32_bf16 a[188:191], a[192:195], v[92:95], a[188:191]
ds_read_b64_tr_b16 a[156:157], v233 offset:12416
v_fma_f32 v245, v9, s27, v242
v_fma_f32 v246, v10, s27, v242
v_mfma_f32_16x16x32_bf16 v[104:107], a[128:131], v[88:91], v[104:107]
ds_read_b64_tr_b16 a[158:159], v233 offset:18560
ds_read_b64_tr_b16 a[160:161], v230 offset:12544
v_fma_f32 v247, v11, s27, v242
v_mfma_f32_16x16x32_bf16 v[108:111], a[128:131], v[92:95], v[108:111]
ds_read_b64_tr_b16 a[162:163], v230 offset:18688
v_exp_f32 v244, v244
v_mfma_f32_16x16x32_bf16 v[120:123], a[132:135], v[88:91], v[120:123]
ds_read_b64_tr_b16 a[164:165], v231 offset:12544
v_mfma_f32_16x16x32_bf16 v[124:127], a[132:135], v[92:95], v[124:127]
ds_read_b64_tr_b16 a[166:167], v231 offset:18688
ds_read_b64_tr_b16 a[168:169], v232 offset:12544
v_mfma_f32_16x16x32_bf16 v[136:139], a[136:139], v[88:91], v[136:139]
ds_read_b64_tr_b16 a[170:171], v232 offset:18688
v_exp_f32 v245, v245
v_mfma_f32_16x16x32_bf16 v[140:143], a[136:139], v[92:95], v[140:143]
ds_read_b64_tr_b16 a[172:173], v233 offset:12544
v_cvt_pk_bf16_f32 v72, v244, v245
v_mfma_f32_16x16x32_bf16 v[152:155], a[140:143], v[88:91], v[152:155]
ds_read_b64_tr_b16 a[174:175], v233 offset:18688
v_exp_f32 v246, v246
v_mfma_f32_16x16x32_bf16 v[156:159], a[140:143], v[92:95], v[156:159]
v_xor_b32 v230, 24576, v230
v_mfma_f32_16x16x32_bf16 v[168:171], a[144:147], v[88:91], v[168:171]
v_xor_b32 v231, 24576, v231
v_exp_f32 v247, v247
v_mfma_f32_16x16x32_bf16 v[172:175], a[144:147], v[92:95], v[172:175]
v_xor_b32 v232, 24576, v232
s_add_u32 s57, s26, 200704
buffer_store_dwordx4 v[160:163], v255, s[20:23], s57 offen offset:0 sc0 sc1
v_mfma_f32_16x16x32_bf16 v[184:187], a[148:151], v[88:91], v[184:187]
v_xor_b32 v233, 24576, v233
v_mfma_f32_16x16x32_bf16 v[188:191], a[148:151], v[92:95], v[188:191]
v_cvt_pk_bf16_f32 v73, v246, v247
v_fma_f32 v244, v12, s27, v243
s_waitcnt lgkmcnt(0)
v_mfma_f32_16x16x32_bf16 v[200:203], a[152:155], v[88:91], v[200:203]
ds_read_b128 a[128:131], v228 offset:12288
v_fma_f32 v245, v13, s27, v243
v_mfma_f32_16x16x32_bf16 v[204:207], a[152:155], v[92:95], v[204:207]
ds_read_b128 a[132:135], v229 offset:12288
v_exp_f32 v244, v244
v_mfma_f32_16x16x32_bf16 v[216:219], a[156:159], v[88:91], v[216:219]
ds_read_b128 a[136:139], v228 offset:12416
v_mfma_f32_16x16x32_bf16 v[220:223], a[156:159], v[92:95], v[220:223]
ds_read_b128 a[140:143], v229 offset:12416
v_exp_f32 v245, v245
v_mfma_f32_16x16x32_bf16 a[200:203], a[160:163], v[88:91], a[200:203]
ds_read_b128 a[144:147], v228 offset:12544
v_cvt_pk_bf16_f32 v76, v244, v245
v_mfma_f32_16x16x32_bf16 a[204:207], a[160:163], v[92:95], a[204:207]
ds_read_b128 a[148:151], v229 offset:12544
v_fma_f32 v246, v14, s27, v243
v_mfma_f32_16x16x32_bf16 a[216:219], a[164:167], v[88:91], a[216:219]
v_fma_f32 v247, v15, s27, v243
v_exp_f32 v246, v246
v_mfma_f32_16x16x32_bf16 a[220:223], a[164:167], v[92:95], a[220:223]
v_mfma_f32_16x16x32_bf16 a[232:235], a[168:171], v[88:91], a[232:235]
v_exp_f32 v247, v247
v_mfma_f32_16x16x32_bf16 a[236:239], a[168:171], v[92:95], a[236:239]
buffer_store_dwordx4 v[176:179], v255, s[20:23], s57 offen offset:1024 sc0 sc1
v_cvt_pk_bf16_f32 v77, v246, v247
v_mfma_f32_16x16x32_bf16 a[248:251], a[172:175], v[88:91], a[248:251]
v_fma_f32 v244, v24, s27, v242
v_fma_f32 v245, v25, s27, v242
v_mfma_f32_16x16x32_bf16 a[252:255], a[172:175], v[92:95], a[252:255]
v_exp_f32 v244, v244
s_waitcnt lgkmcnt(0)
v_mfma_f32_16x16x32_bf16 v[40:43], a[128:131], a[48:51], 0
ds_read_b128 a[152:155], v228 offset:18432
v_exp_f32 v245, v245
v_mfma_f32_16x16x32_bf16 v[40:43], a[132:135], a[52:55], v[40:43]
ds_read_b128 a[156:159], v229 offset:18432
v_mfma_f32_16x16x32_bf16 v[40:43], a[136:139], a[56:59], v[40:43]
ds_read_b128 a[160:163], v228 offset:18560
v_cvt_pk_bf16_f32 v74, v244, v245
v_mfma_f32_16x16x32_bf16 v[40:43], a[140:143], a[60:63], v[40:43]
ds_read_b128 a[164:167], v229 offset:18560
v_fma_f32 v246, v26, s27, v242
v_mfma_f32_16x16x32_bf16 v[40:43], a[144:147], a[112:115], v[40:43]
ds_read_b128 a[168:171], v228 offset:18688
v_fma_f32 v247, v27, s27, v242
v_mfma_f32_16x16x32_bf16 v[40:43], a[148:151], a[116:119], v[40:43]
v_exp_f32 v246, v246
v_mfma_f32_16x16x32_bf16 v[44:47], a[128:131], a[72:75], 0
ds_read_b128 a[172:175], v229 offset:18688
v_exp_f32 v247, v247
v_mfma_f32_16x16x32_bf16 v[44:47], a[132:135], a[76:79], v[44:47]
v_cvt_pk_bf16_f32 v75, v246, v247
v_mfma_f32_16x16x32_bf16 v[44:47], a[136:139], a[80:83], v[44:47]
v_xor_b32 v228, 24576, v228
v_fma_f32 v244, v28, s27, v243
v_mfma_f32_16x16x32_bf16 v[44:47], a[140:143], a[84:87], v[44:47]
s_add_u32 s56, s26, 202752
buffer_store_dwordx4 v[100:103], v255, s[20:23], s56 offen offset:0 sc0 sc1
v_mfma_f32_16x16x32_bf16 v[44:47], a[144:147], a[120:123], v[44:47]
v_xor_b32 v229, 24576, v229
v_fma_f32 v245, v29, s27, v243
v_mfma_f32_16x16x32_bf16 v[44:47], a[148:151], a[124:127], v[44:47]
v_exp_f32 v244, v244
s_waitcnt lgkmcnt(0)
v_mfma_f32_16x16x32_bf16 v[56:59], a[152:155], a[48:51], 0
ds_read_b64_tr_b16 a[128:129], v230 offset:0
ds_read_b64_tr_b16 a[130:131], v230 offset:6144
v_fma_f32 v246, v30, s27, v243
v_mfma_f32_16x16x32_bf16 v[56:59], a[156:159], a[52:55], v[56:59]
ds_read_b64_tr_b16 a[132:133], v231 offset:0
v_exp_f32 v245, v245
v_mfma_f32_16x16x32_bf16 v[56:59], a[160:163], a[56:59], v[56:59]
ds_read_b64_tr_b16 a[134:135], v231 offset:6144
ds_read_b64_tr_b16 a[136:137], v232 offset:0
v_mfma_f32_16x16x32_bf16 v[56:59], a[164:167], a[60:63], v[56:59]
ds_read_b64_tr_b16 a[138:139], v232 offset:6144
v_cvt_pk_bf16_f32 v78, v244, v245
v_mfma_f32_16x16x32_bf16 v[56:59], a[168:171], a[112:115], v[56:59]
ds_read_b64_tr_b16 a[140:141], v233 offset:0
v_fma_f32 v247, v31, s27, v243
v_mfma_f32_16x16x32_bf16 v[56:59], a[172:175], a[116:119], v[56:59]
ds_read_b64_tr_b16 a[142:143], v233 offset:6144
ds_read_b64_tr_b16 a[144:145], v230 offset:128
v_mfma_f32_16x16x32_bf16 v[60:63], a[152:155], a[72:75], 0
ds_read_b64_tr_b16 a[146:147], v230 offset:6272
v_exp_f32 v246, v246
v_mfma_f32_16x16x32_bf16 v[60:63], a[156:159], a[76:79], v[60:63]
ds_read_b64_tr_b16 a[148:149], v231 offset:128
v_exp_f32 v247, v247
v_mfma_f32_16x16x32_bf16 v[60:63], a[160:163], a[80:83], v[60:63]
ds_read_b64_tr_b16 a[150:151], v231 offset:6272
v_mfma_f32_16x16x32_bf16 v[60:63], a[164:167], a[84:87], v[60:63]
v_cvt_pk_bf16_f32 v79, v246, v247
v_mfma_f32_16x16x32_bf16 v[60:63], a[168:171], a[120:123], v[60:63]
buffer_store_dwordx4 v[116:119], v255, s[20:23], s56 offen offset:1024 sc0 sc1
v_mfma_f32_16x16x32_bf16 v[60:63], a[172:175], a[124:127], v[60:63]
s_waitcnt lgkmcnt(0)
v_mfma_f32_16x16x32_bf16 a[184:187], a[192:195], v[72:75], a[184:187]
ds_read_b64_tr_b16 a[152:153], v232 offset:128
ds_read_b64_tr_b16 a[154:155], v232 offset:6272
v_fma_f32 v244, v40, s27, v242
v_fma_f32 v245, v41, s27, v242
v_mfma_f32_16x16x32_bf16 a[188:191], a[192:195], v[76:79], a[188:191]
ds_read_b64_tr_b16 a[156:157], v233 offset:128
v_exp_f32 v244, v244
v_fma_f32 v246, v42, s27, v242
v_mfma_f32_16x16x32_bf16 v[104:107], a[128:131], v[72:75], v[104:107]
ds_read_b64_tr_b16 a[158:159], v233 offset:6272
ds_read_b64_tr_b16 a[160:161], v230 offset:256
v_exp_f32 v245, v245
v_mfma_f32_16x16x32_bf16 v[108:111], a[128:131], v[76:79], v[108:111]
ds_read_b64_tr_b16 a[162:163], v230 offset:6400
v_cvt_pk_bf16_f32 v88, v244, v245
v_fma_f32 v247, v43, s27, v242
v_mfma_f32_16x16x32_bf16 v[120:123], a[132:135], v[72:75], v[120:123]
ds_read_b64_tr_b16 a[164:165], v231 offset:256
v_exp_f32 v246, v246
v_fma_f32 v244, v44, s27, v243
v_mfma_f32_16x16x32_bf16 v[124:127], a[132:135], v[76:79], v[124:127]
ds_read_b64_tr_b16 a[166:167], v231 offset:6400
ds_read_b64_tr_b16 a[168:169], v232 offset:256
v_exp_f32 v247, v247
v_mfma_f32_16x16x32_bf16 v[136:139], a[136:139], v[72:75], v[136:139]
ds_read_b64_tr_b16 a[170:171], v232 offset:6400
v_cvt_pk_bf16_f32 v89, v246, v247
v_mfma_f32_16x16x32_bf16 v[140:143], a[136:139], v[76:79], v[140:143]
ds_read_b64_tr_b16 a[172:173], v233 offset:256
v_fma_f32 v245, v45, s27, v243
v_exp_f32 v244, v244
v_mfma_f32_16x16x32_bf16 v[152:155], a[140:143], v[72:75], v[152:155]
ds_read_b64_tr_b16 a[174:175], v233 offset:6400
v_exp_f32 v245, v245
v_mfma_f32_16x16x32_bf16 v[156:159], a[140:143], v[76:79], v[156:159]
v_cvt_pk_bf16_f32 v92, v244, v245
v_fma_f32 v246, v46, s27, v243
v_fma_f32 v247, v47, s27, v243
v_mfma_f32_16x16x32_bf16 v[168:171], a[144:147], v[72:75], v[168:171]
buffer_store_dwordx4 v[132:135], v255, s[20:23], s56 offen offset:2048 sc0 sc1
v_exp_f32 v246, v246
v_fma_f32 v244, v56, s27, v242
v_mfma_f32_16x16x32_bf16 v[172:175], a[144:147], v[76:79], v[172:175]
v_exp_f32 v247, v247
v_fma_f32 v245, v57, s27, v242
v_mfma_f32_16x16x32_bf16 v[184:187], a[148:151], v[72:75], v[184:187]
buffer_store_dwordx4 v[148:151], v255, s[20:23], s56 offen offset:3072 sc0 sc1
v_cvt_pk_bf16_f32 v93, v246, v247
v_exp_f32 v244, v244
v_mfma_f32_16x16x32_bf16 v[188:191], a[148:151], v[76:79], v[188:191]
v_exp_f32 v245, v245
s_waitcnt lgkmcnt(0)
v_mfma_f32_16x16x32_bf16 v[200:203], a[152:155], v[72:75], v[200:203]
ds_read_b64_tr_b16 a[128:129], v230 offset:12288
ds_read_b64_tr_b16 a[130:131], v230 offset:18432
v_cvt_pk_bf16_f32 v90, v244, v245
v_fma_f32 v246, v58, s27, v242
v_mfma_f32_16x16x32_bf16 v[204:207], a[152:155], v[76:79], v[204:207]
ds_read_b64_tr_b16 a[132:133], v231 offset:12288
v_fma_f32 v247, v59, s27, v242
v_exp_f32 v246, v246
v_mfma_f32_16x16x32_bf16 v[216:219], a[156:159], v[72:75], v[216:219]
ds_read_b64_tr_b16 a[134:135], v231 offset:18432
ds_read_b64_tr_b16 a[136:137], v232 offset:12288
v_exp_f32 v247, v247
v_mfma_f32_16x16x32_bf16 v[220:223], a[156:159], v[76:79], v[220:223]
ds_read_b64_tr_b16 a[138:139], v232 offset:18432
v_cvt_pk_bf16_f32 v91, v246, v247
v_mfma_f32_16x16x32_bf16 a[200:203], a[160:163], v[72:75], a[200:203]
ds_read_b64_tr_b16 a[140:141], v233 offset:12288
v_fma_f32 v244, v60, s27, v243
v_fma_f32 v245, v61, s27, v243
v_mfma_f32_16x16x32_bf16 a[204:207], a[160:163], v[76:79], a[204:207]
ds_read_b64_tr_b16 a[142:143], v233 offset:18432
ds_read_b64_tr_b16 a[144:145], v230 offset:12416
v_exp_f32 v244, v244
v_mfma_f32_16x16x32_bf16 a[216:219], a[164:167], v[72:75], a[216:219]
ds_read_b64_tr_b16 a[146:147], v230 offset:18560
v_exp_f32 v245, v245
v_mfma_f32_16x16x32_bf16 a[220:223], a[164:167], v[76:79], a[220:223]
ds_read_b64_tr_b16 a[148:149], v231 offset:12416
v_cvt_pk_bf16_f32 v94, v244, v245
v_fma_f32 v246, v62, s27, v243
v_mfma_f32_16x16x32_bf16 a[232:235], a[168:171], v[72:75], a[232:235]
ds_read_b64_tr_b16 a[150:151], v231 offset:18560
v_fma_f32 v247, v63, s27, v243
v_exp_f32 v246, v246
v_mfma_f32_16x16x32_bf16 a[236:239], a[168:171], v[76:79], a[236:239]
s_add_u32 s57, s26, 206848
buffer_store_dwordx4 v[164:167], v255, s[20:23], s57 offen offset:0 sc0 sc1
v_exp_f32 v247, v247
v_mfma_f32_16x16x32_bf16 a[248:251], a[172:175], v[72:75], a[248:251]
v_cvt_pk_bf16_f32 v95, v246, v247
v_mfma_f32_16x16x32_bf16 a[252:255], a[172:175], v[76:79], a[252:255]
buffer_store_dwordx4 v[180:183], v255, s[20:23], s57 offen offset:1024 sc0 sc1
s_waitcnt lgkmcnt(0)
v_mfma_f32_16x16x32_bf16 a[184:187], a[192:195], v[88:91], a[184:187]
ds_read_b64_tr_b16 a[152:153], v232 offset:12416
ds_read_b64_tr_b16 a[154:155], v232 offset:18560
v_mfma_f32_16x16x32_bf16 a[188:191], a[192:195], v[92:95], a[188:191]
ds_read_b64_tr_b16 a[156:157], v233 offset:12416
v_mfma_f32_16x16x32_bf16 v[104:107], a[128:131], v[88:91], v[104:107]
ds_read_b64_tr_b16 a[158:159], v233 offset:18560
ds_read_b64_tr_b16 a[160:161], v230 offset:12544
v_mfma_f32_16x16x32_bf16 v[108:111], a[128:131], v[92:95], v[108:111]
ds_read_b64_tr_b16 a[162:163], v230 offset:18688
v_mfma_f32_16x16x32_bf16 v[120:123], a[132:135], v[88:91], v[120:123]
ds_read_b64_tr_b16 a[164:165], v231 offset:12544
v_mfma_f32_16x16x32_bf16 v[124:127], a[132:135], v[92:95], v[124:127]
ds_read_b64_tr_b16 a[166:167], v231 offset:18688
ds_read_b64_tr_b16 a[168:169], v232 offset:12544
v_mfma_f32_16x16x32_bf16 v[136:139], a[136:139], v[88:91], v[136:139]
ds_read_b64_tr_b16 a[170:171], v232 offset:18688
v_mfma_f32_16x16x32_bf16 v[140:143], a[136:139], v[92:95], v[140:143]
ds_read_b64_tr_b16 a[172:173], v233 offset:12544
v_mfma_f32_16x16x32_bf16 v[152:155], a[140:143], v[88:91], v[152:155]
ds_read_b64_tr_b16 a[174:175], v233 offset:18688
v_mfma_f32_16x16x32_bf16 v[156:159], a[140:143], v[92:95], v[156:159]
v_mfma_f32_16x16x32_bf16 v[168:171], a[144:147], v[88:91], v[168:171]
v_mfma_f32_16x16x32_bf16 v[172:175], a[144:147], v[92:95], v[172:175]
v_mfma_f32_16x16x32_bf16 v[184:187], a[148:151], v[88:91], v[184:187]
v_mfma_f32_16x16x32_bf16 v[188:191], a[148:151], v[92:95], v[188:191]
s_waitcnt lgkmcnt(0)
v_mfma_f32_16x16x32_bf16 v[200:203], a[152:155], v[88:91], v[200:203]
v_mfma_f32_16x16x32_bf16 v[204:207], a[152:155], v[92:95], v[204:207]
v_mfma_f32_16x16x32_bf16 v[216:219], a[156:159], v[88:91], v[216:219]
v_mfma_f32_16x16x32_bf16 v[220:223], a[156:159], v[92:95], v[220:223]
v_mfma_f32_16x16x32_bf16 a[200:203], a[160:163], v[88:91], a[200:203]
v_mfma_f32_16x16x32_bf16 a[204:207], a[160:163], v[92:95], a[204:207]
v_mfma_f32_16x16x32_bf16 a[216:219], a[164:167], v[88:91], a[216:219]
v_mfma_f32_16x16x32_bf16 a[220:223], a[164:167], v[92:95], a[220:223]
v_mfma_f32_16x16x32_bf16 a[232:235], a[168:171], v[88:91], a[232:235]
v_mfma_f32_16x16x32_bf16 a[236:239], a[168:171], v[92:95], a[236:239]
v_mfma_f32_16x16x32_bf16 a[248:251], a[172:175], v[88:91], a[248:251]
v_mfma_f32_16x16x32_bf16 a[252:255], a[172:175], v[92:95], a[252:255]
s_nop 7
; ---- epilogue 1
v_mbcnt_lo_u32_b32 v247, -1, 0
v_mbcnt_hi_u32_b32 v247, -1, v247
v_and_b32 v246, 15, v247
v_mul_u32_u24 v244, 384, v246
v_add_u32 v244, s30, v244
v_and_b32 v246, 14, v246
v_lshrrev_b32 v245, 4, v247
v_xor_b32 v246, v246, v245
v_xor_b32 v250, 0, v246
v_lshl_add_u32 v250, v250, 3, v244
v_xor_b32 v251, 4, v246
v_lshl_add_u32 v251, v251, 3, v244
v_xor_b32 v252, 8, v246
v_lshl_add_u32 v252, v252, 3, v244
v_xor_b32 v253, 12, v246
v_lshl_add_u32 v253, v253, 3, v244
v_xor_b32 v249, s31, v234
v_add_u32 v249, s30, v249
v_xor_b32 v254, s31, v235
v_add_u32 v254, s30, v254
v_xor_b32 v255, s31, v236
v_add_u32 v255, s30, v255
v_lshlrev_b32 v247, 4, v247
v_accvgpr_read_b32 v244, a184
v_cmp_nge_f32 vcc, 0x5b800000, v244
v_rcp_f32 v244, v244
s_nop 1
s_or_b64 s[98:99], s[98:99], vcc
s_nop 0
v_pk_mul_f32 v[104:105], v[104:105], v[244:245] op_sel_hi:[1,0]
v_pk_mul_f32 v[106:107], v[106:107], v[244:245] op_sel_hi:[1,0]
v_pk_mul_f32 v[120:121], v[120:121], v[244:245] op_sel_hi:[1,0]
v_pk_mul_f32 v[122:123], v[122:123], v[244:245] op_sel_hi:[1,0]
v_cvt_pk_bf16_f32 v104, v104, v105
v_cvt_pk_bf16_f32 v105, v106, v107
v_cvt_pk_bf16_f32 v120, v120, v121
v_cvt_pk_bf16_f32 v121, v122, v123
ds_write_b64 v250, v[104:105] offset:0
ds_write_b64 v251, v[120:121] offset:0
v_pk_mul_f32 v[136:137], v[136:137], v[244:245] op_sel_hi:[1,0]
v_pk_mul_f32 v[138:139], v[138:139], v[244:245] op_sel_hi:[1,0]
v_pk_mul_f32 v[152:153], v[152:153], v[244:245] op_sel_hi:[1,0]
v_pk_mul_f32 v[154:155], v[154:155], v[244:245] op_sel_hi:[1,0]
v_cvt_pk_bf16_f32 v136, v136, v137
v_cvt_pk_bf16_f32 v137, v138, v139
v_cvt_pk_bf16_f32 v152, v152, v153
v_cvt_pk_bf16_f32 v153, v154, v155
ds_write_b64 v252, v[136:137] offset:0
ds_write_b64 v253, v[152:153] offset:0
v_pk_mul_f32 v[168:169], v[168:169], v[244:245] op_sel_hi:[1,0]
v_pk_mul_f32 v[170:171], v[170:171], v[244:245] op_sel_hi:[1,0]
v_pk_mul_f32 v[184:185], v[184:185], v[244:245] op_sel_hi:[1,0]
v_pk_mul_f32 v[186:187], v[186:187], v[244:245] op_sel_hi:[1,0]
v_cvt_pk_bf16_f32 v168, v168, v169
v_cvt_pk_bf16_f32 v169, v170, v171
v_cvt_pk_bf16_f32 v184, v184, v185
v_cvt_pk_bf16_f32 v185, v186, v187
ds_write_b64 v250, v[168:169] offset:128
ds_write_b64 v251, v[184:185] offset:128
v_pk_mul_f32 v[200:201], v[200:201], v[244:245] op_sel_hi:[1,0]
v_pk_mul_f32 v[202:203], v[202:203], v[244:245] op_sel_hi:[1,0]
v_pk_mul_f32 v[216:217], v[216:217], v[244:245] op_sel_hi:[1,0]
v_pk_mul_f32 v[218:219], v[218:219], v[244:245] op_sel_hi:[1,0]
v_cvt_pk_bf16_f32 v200, v200, v201
v_cvt_pk_bf16_f32 v201, v202, v203
v_cvt_pk_bf16_f32 v216, v216, v217
v_cvt_pk_bf16_f32 v217, v218, v219
ds_write_b64 v252, v[200:201] offset:128
ds_write_b64 v253, v[216:217] offset:128
v_accvgpr_read_b32 v184, a200
v_accvgpr_read_b32 v185, a201
v_accvgpr_read_b32 v186, a202
v_accvgpr_read_b32 v187, a203
v_accvgpr_read_b32 v168, a216
v_accvgpr_read_b32 v169, a217
v_accvgpr_read_b32 v170, a218
v_accvgpr_read_b32 v171, a219
v_accvgpr_read_b32 v152, a232
v_accvgpr_read_b32 v153, a233
v_accvgpr_read_b32 v154, a234
v_accvgpr_read_b32 v155, a235
v_accvgpr_read_b32 v136, a248
v_accvgpr_read_b32 v137, a249
v_accvgpr_read_b32 v138, a250
v_accvgpr_read_b32 v139, a251
v_pk_mul_f32 v[184:185], v[184:185], v[244:245] op_sel_hi:[1,0]
v_pk_mul_f32 v[186:187], v[186:187], v[244:245] op_sel_hi:[1,0]
v_pk_mul_f32 v[168:169], v[168:169], v[244:245] op_sel_hi:[1,0]
v_pk_mul_f32 v[170:171], v[170:171], v[244:245] op_sel_hi:[1,0]
v_pk_mul_f32 v[152:153], v[152:153], v[244:245] op_sel_hi:[1,0]
v_pk_mul_f32 v[154:155], v[154:155], v[244:245] op_sel_hi:[1,0]
v_pk_mul_f32 v[136:137], v[136:137], v[244:245] op_sel_hi:[1,0]
v_pk_mul_f32 v[138:139], v[138:139], v[244:245] op_sel_hi:[1,0]
v_cvt_pk_bf16_f32 v184, v184, v185
v_cvt_pk_bf16_f32 v185, v186, v187
v_cvt_pk_bf16_f32 v168, v168, v169
v_cvt_pk_bf16_f32 v169, v170, v171
v_cvt_pk_bf16_f32 v152, v152, v153
v_cvt_pk_bf16_f32 v153, v154, v155
v_cvt_pk_bf16_f32 v136, v136, v137
v_cvt_pk_bf16_f32 v137, v138, v139
ds_write_b64 v250, v[184:185] offset:256
ds_write_b64 v251, v[168:169] offset:256
ds_write_b64 v252, v[152:153] offset:256
ds_write_b64 v253, v[136:137] offset:256
ds_read_b128 v[104:107], v249 offset:0
ds_read_b128 v[120:123], v254 offset:1024
ds_read_b128 v[136:139], v255 offset:2048
v_xor_b32 v246, 64, v249
ds_read_b128 v[152:155], v246 offset:3072
v_xor_b32 v246, 64, v254
ds_read_b128 v[168:171], v246 offset:4096
v_xor_b32 v246, 64, v255
ds_read_b128 v[184:187], v246 offset:5120
v_accvgpr_read_b32 v244, a188
v_cmp_nge_f32 vcc, 0x5b800000, v244
v_rcp_f32 v244, v244
s_nop 1
s_or_b64 s[98:99], s[98:99], vcc
s_nop 0
v_pk_mul_f32 v[108:109], v[108:109], v[244:245] op_sel_hi:[1,0]
v_pk_mul_f32 v[110:111], v[110:111], v[244:245] op_sel_hi:[1,0]
v_pk_mul_f32 v[124:125], v[124:125], v[244:245] op_sel_hi:[1,0]
v_pk_mul_f32 v[126:127], v[126:127], v[244:245] op_sel_hi:[1,0]
v_cvt_pk_bf16_f32 v108, v108, v109
v_cvt_pk_bf16_f32 v109, v110, v111
v_cvt_pk_bf16_f32 v124, v124, v125
v_cvt_pk_bf16_f32 v125, v126, v127
ds_write_b64 v250, v[108:109] offset:6144
ds_write_b64 v251, v[124:125] offset:6144
v_pk_mul_f32 v[140:141], v[140:141], v[244:245] op_sel_hi:[1,0]
v_pk_mul_f32 v[142:143], v[142:143], v[244:245] op_sel_hi:[1,0]
v_pk_mul_f32 v[156:157], v[156:157], v[244:245] op_sel_hi:[1,0]
v_pk_mul_f32 v[158:159], v[158:159], v[244:245] op_sel_hi:[1,0]
s_waitcnt lgkmcnt(2)
s_add_u32 s42, s26, 208896
s_add_u32 s43, s26, 212992
v_cvt_pk_bf16_f32 v140, v140, v141
v_cvt_pk_bf16_f32 v141, v142, v143
v_cvt_pk_bf16_f32 v156, v156, v157
v_cvt_pk_bf16_f32 v157, v158, v159
buffer_store_dwordx4 v[104:107], v247, s[20:23], s42 offen offset:0 sc0 sc1
ds_write_b64 v252, v[140:141] offset:6144
ds_write_b64 v253, v[156:157] offset:6144
v_pk_mul_f32 v[172:173], v[172:173], v[244:245] op_sel_hi:[1,0]
v_pk_mul_f32 v[174:175], v[174:175], v[244:245] op_sel_hi:[1,0]
v_pk_mul_f32 v[188:189], v[188:189], v[244:245] op_sel_hi:[1,0]
v_pk_mul_f32 v[190:191], v[190:191], v[244:245] op_sel_hi:[1,0]
v_cvt_pk_bf16_f32 v172, v172, v173
v_cvt_pk_bf16_f32 v173, v174, v175
v_cvt_pk_bf16_f32 v188, v188, v189
v_cvt_pk_bf16_f32 v189, v190, v191
buffer_store_dwordx4 v[120:123], v247, s[20:23], s42 offen offset:1024 sc0 sc1
ds_write_b64 v250, v[172:173] offset:6272
ds_write_b64 v251, v[188:189] offset:6272
v_pk_mul_f32 v[204:205], v[204:205], v[244:245] op_sel_hi:[1,0]
v_pk_mul_f32 v[206:207], v[206:207], v[244:245] op_sel_hi:[1,0]
v_pk_mul_f32 v[220:221], v[220:221], v[244:245] op_sel_hi:[1,0]
v_pk_mul_f32 v[222:223], v[222:223], v[244:245] op_sel_hi:[1,0]
v_cvt_pk_bf16_f32 v204, v204, v205
v_cvt_pk_bf16_f32 v205, v206, v207
v_cvt_pk_bf16_f32 v220, v220, v221
v_cvt_pk_bf16_f32 v221, v222, v223
buffer_store_dwordx4 v[136:139], v247, s[20:23], s42 offen offset:2048 sc0 sc1
ds_write_b64 v252, v[204:205] offset:6272
ds_write_b64 v253, v[220:221] offset:6272
v_accvgpr_read_b32 v188, a204
v_accvgpr_read_b32 v189, a205
v_accvgpr_read_b32 v190, a206
v_accvgpr_read_b32 v191, a207
v_accvgpr_read_b32 v172, a220
v_accvgpr_read_b32 v173, a221
v_accvgpr_read_b32 v174, a222
v_accvgpr_read_b32 v175, a223
buffer_store_dwordx4 v[152:155], v247, s[20:23], s42 offen offset:3072 sc0 sc1
v_accvgpr_read_b32 v156, a236
v_accvgpr_read_b32 v157, a237
v_accvgpr_read_b32 v158, a238
v_accvgpr_read_b32 v159, a239
v_accvgpr_read_b32 v140, a252
v_accvgpr_read_b32 v141, a253
v_accvgpr_read_b32 v142, a254
v_accvgpr_read_b32 v143, a255
v_pk_mul_f32 v[188:189], v[188:189], v[244:245] op_sel_hi:[1,0]
v_pk_mul_f32 v[190:191], v[190:191], v[244:245] op_sel_hi:[1,0]
buffer_store_dwordx4 v[168:171], v247, s[20:23], s43 offen offset:0 sc0 sc1
v_pk_mul_f32 v[172:173], v[172:173], v[244:245] op_sel_hi:[1,0]
v_pk_mul_f32 v[174:175], v[174:175], v[244:245] op_sel_hi:[1,0]
v_pk_mul_f32 v[156:157], v[156:157], v[244:245] op_sel_hi:[1,0]
v_pk_mul_f32 v[158:159], v[158:159], v[244:245] op_sel_hi:[1,0]
v_pk_mul_f32 v[140:141], v[140:141], v[244:245] op_sel_hi:[1,0]
v_pk_mul_f32 v[142:143], v[142:143], v[244:245] op_sel_hi:[1,0]
v_cvt_pk_bf16_f32 v188, v188, v189
v_cvt_pk_bf16_f32 v189, v190, v191
v_cvt_pk_bf16_f32 v172, v172, v173
buffer_store_dwordx4 v[184:187], v247, s[20:23], s43 offen offset:1024 sc0 sc1
v_cvt_pk_bf16_f32 v173, v174, v175
v_cvt_pk_bf16_f32 v156, v156, v157
v_cvt_pk_bf16_f32 v157, v158, v159
v_cvt_pk_bf16_f32 v140, v140, v141
v_cvt_pk_bf16_f32 v141, v142, v143
ds_write_b64 v250, v[188:189] offset:6400
ds_write_b64 v251, v[172:173] offset:6400
ds_write_b64 v252, v[156:157] offset:6400
ds_write_b64 v253, v[140:141] offset:6400
ds_read_b128 v[108:111], v249 offset:6144
ds_read_b128 v[124:127], v254 offset:7168
ds_read_b128 v[140:143], v255 offset:8192
v_xor_b32 v246, 64, v249
ds_read_b128 v[156:159], v246 offset:9216
v_xor_b32 v246, 64, v254
ds_read_b128 v[172:175], v246 offset:10240
v_xor_b32 v246, 64, v255
ds_read_b128 v[188:191], v246 offset:11264
s_waitcnt lgkmcnt(0)
s_add_u32 s42, s26, 215040
s_add_u32 s43, s26, 219136
buffer_store_dwordx4 v[108:111], v247, s[20:23], s42 offen offset:0 sc0 sc1
buffer_store_dwordx4 v[124:127], v247, s[20:23], s42 offen offset:1024 sc0 sc1
buffer_store_dwordx4 v[140:143], v247, s[20:23], s42 offen offset:2048 sc0 sc1
buffer_store_dwordx4 v[156:159], v247, s[20:23], s42 offen offset:3072 sc0 sc1
buffer_store_dwordx4 v[172:175], v247, s[20:23], s43 offen offset:0 sc0 sc1
buffer_store_dwordx4 v[188:191], v247, s[20:23], s43 offen offset:1024 sc0 sc1
s_waitcnt vmcnt(0)
s_branch END_%=
CEND_%=:
END_%=:
)ASM"
               : "+{s[0:1]}"(karg), "+{s2}"(bid), "+{v0}"(tid) ATTN_EXTRA_OUT
               :
               : "v1", "v2", "v3", "v4", "v5", "v6", "v7", "v8", "v9", "v10", "v11", "v12", "v13", "v14", "v15", "v16", "v17", "v18", "v19", "v20", "v21", "v22", "v23", "v24", "v25", "v26", "v27", "v28", "v29", "v30", "v31", "v32", "v33", "v34", "v35", "v36", "v37", "v38", "v39", "v40", "v41", "v42", "v43", "v44", "v45", "v46", "v47", "v48", "v49", "v50", "v51", "v52", "v53", "v54", "v55", "v56", "v57", "v58", "v59", "v60", "v61", "v62", "v63", "v64", "v65", "v66", "v67", "v68", "v69", "v70", "v71", "v72", "v73", "v74", "v75", "v76", "v77", "v78", "v79", "v80", "v81", "v82", "v83", "v84", "v85", "v86", "v87", "v88", "v89", "v90", "v91", "v92", "v93", "v94", "v95", "v96", "v97", "v98", "v99", "v100", "v101", "v102", "v103", "v104", "v105", "v106", "v107", "v108", "v109", "v110", "v111", "v112", "v113", "v114", "v115", "v116", "v117", "v118", "v119", "v120", "v121", "v122", "v123", "v124", "v125", "v126", "v127", "v128", "v129", "v130", "v131", "v132", "v133", "v134", "v135", "v136", "v137", "v138", "v139", "v140", "v141", "v142", "v143", "v144", "v145", "v146", "v147", "v148", "v149", "v150", "v151", "v152", "v153", "v154", "v155", "v156", "v157", "v158", "v159", "v160", "v161", "v162", "v163", "v164", "v165", "v166", "v167", "v168", "v169", "v170", "v171", "v172", "v173", "v174", "v175", "v176", "v177", "v178", "v179", "v180", "v181", "v182", "v183", "v184", "v185", "v186", "v187", "v188", "v189", "v190", "v191", "v192", "v193", "v194", "v195", "v196", "v197", "v198", "v199", "v200", "v201", "v202", "v203", "v204", "v205", "v206", "v207", "v208", "v209", "v210", "v211", "v212", "v213", "v214", "v215", "v216", "v217", "v218", "v219", "v220", "v221", "v222", "v223", "v224", "v225", "v226", "v227", "v228", "v229", "v230", "v231", "v232", "v233", "v234", "v235", "v236", "v237", "v238", "v239", "v240", "v241", "v242", "v243", "v244", "v245", "v246", "v247", "v248", "v249", "v250", "v251", "v252", "v253", "v254", "v255", "a0", "a1", "a2", "a3", "a4", "a5", "a6", "a7", "a8", "a9", "a10", "a11", "a12", "a13", "a14", "a15", "a16", "a17", "a18", "a19", "a20", "a21", "a22", "a23", "a24", "a25", "a26", "a27", "a28", "a29", "a30", "a31", "a32", "a33", "a34", "a35", "a36", "a37", "a38", "a39", "a40", "a41", "a42", "a43", "a44", "a45", "a46", "a47", "a48", "a49", "a50", "a51", "a52", "a53", "a54", "a55", "a56", "a57", "a58", "a59", "a60", "a61", "a62", "a63", "a64", "a65", "a66", "a67", "a68", "a69", "a70", "a71", "a72", "a73", "a74", "a75", "a76", "a77", "a78", "a79", "a80", "a81", "a82", "a83", "a84", "a85", "a86", "a87", "a88", "a89", "a90", "a91", "a92", "a93", "a94", "a95", "a96", "a97", "a98", "a99", "a100", "a101", "a102", "a103", "a104", "a105", "a106", "a107", "a108", "a109", "a110", "a111", "a112", "a113", "a114", "a115", "a116", "a117", "a118", "a119", "a120", "a121", "a122", "a123", "a124", "a125", "a126", "a127", "a128", "a129", "a130", "a131", "a132", "a133", "a134", "a135", "a136", "a137", "a138", "a139", "a140", "a141", "a142", "a143", "a144", "a145", "a146", "a147", "a148", "a149", "a150", "a151", "a152", "a153", "a154", "a155", "a156", "a157", "a158", "a159", "a160", "a161", "a162", "a163", "a164", "a165", "a166", "a167", "a168", "a169", "a170", "a171", "a172", "a173", "a174", "a175", "a176", "a177", "a178", "a179", "a180", "a181", "a182", "a183", "a184", "a185", "a186", "a187", "a188", "a189", "a190", "a191", "a192", "a193", "a194", "a195", "a196", "a197", "a198", "a199", "a200", "a201", "a202", "a203", "a204", "a205", "a206", "a207", "a208", "a209", "a210", "a211", "a212", "a213", "a214", "a215", "a216", "a217", "a218", "a219", "a220", "a221", "a222", "a223", "a224", "a225", "a226", "a227", "a228", "a229", "a230", "a231", "a232", "a233", "a234", "a235", "a236", "a237", "a238", "a239", "a240", "a241", "a242", "a243", "a244", "a245", "a246", "a247", "a248", "a249", "a250", "a251", "a252", "a253", "a254", "a255", "s4", "s5", "s6", "s7", "s8", "s9", "s10", "s11", "s12", "s13", "s14", "s15", "s16", "s17", "s18", "s19", "s20", "s21", "s22", "s23", "s24", "s25", "s26", "s27", "s28", "s29", "s30", "s31", "s34", "s35", "s36", "s37", "s38", "s39", "s40", "s41", "s42", "s43", "s44", "s45", "s46", "s47", "s48", "s49", "s50", "s51", "s52", "s53", "s54", "s55", "s56", "s57", "s58", "s59", "s60", "s61", "s62", "s63", "s64", "s65", "s66", "s67", "s68", "s69", "s70", "s71", "s72", "s73", "s74", "s75", "s76", "s77", "s78", "s79", "s80", "s81", "s82", "s83", "s84", "s85", "s86", "s87", "s88", "s89", "s90", "s91", "s92", "s93", "s94", "s95", "vcc", "scc", "memory");
  if (__builtin_expect((bad0 | bad1) != 0ull, 0)) {
    const void* const* ka = (const void* const*)karg;
    unsigned lane;
    asm volatile("v_mbcnt_lo_u32_b32 %0, -1, 0\n\tv_mbcnt_hi_u32_b32 %0, -1, %0" : "=v"(lane));
    const unsigned head = (bid & 7u) * 32u + 2u * (bid >> 4), half = (bid >> 3) & 1u;
#pragma unroll 1
    for (unsigned b = 0; b < 2; ++b) {
      if ((b ? bad1 : bad0) == 0ull) continue;
      const size_t hoff = (size_t)(head + b) * 196608u, roff = hoff + half * 98304u + wave * 24576u;
      attn_exact_rows((const char*)ka[0] + roff, (const char*)ka[1] + hoff, (const char*)ka[2] + hoff, (char*)ka[3] + roff, lane);
    }
  }
}

extern "C" void kernel_launch(void* const* d_in, const int* in_sizes, int n_in, void* d_out, int out_size, void* d_ws, size_t ws_size,
                              hipStream_t stream) {
  (void)in_sizes; (void)n_in; (void)out_size; (void)ws_size;
  (void)hipFuncSetAttribute((const void*)attn_fwd_d192, hipFuncAttributeMaxDynamicSharedMemorySize, 163840);
  hipLaunchKernelGGL(attn_fwd_d192, dim3(256), dim3(256), 163840, stream, d_in[0], d_in[1], d_in[2], d_out, d_ws);
}
```
